```python
import torch

def _softmax(x, axis=-1):
    return torch.softmax(x, dim=axis)
def _gelu(x):
    return torch.nn.functional.gelu(x, approximate="tanh")
def _silu(x):
    return torch.nn.functional.silu(x)
def _sigmoid(x):
    return torch.sigmoid(x)
def _layernorm(x, axis=-1, eps=1e-5):
    m = x.mean(dim=axis, keepdim=True)
    v = x.var(dim=axis, keepdim=True, unbiased=False)
    return (x - m) / torch.sqrt(v + eps)
def _scatter_add(tgt, idx, src, axis=0):
    return tgt.clone().index_add_(axis, idx, src)
def _segment_sum(src, seg, n):
    out = torch.zeros((n,) + src.shape[1:], dtype=src.dtype)
    return out.index_add_(0, seg, src)
def _pad(x, pw):
    flat = tuple(v for pair in reversed(pw) for v in pair)
    return torch.nn.functional.pad(x, flat)

kernel_name = 'parallel_sum__ssm_conv_s4__dense_activation_relu__prompt'

def setup_inputs():

    _w = (lambda a: (lambda t: t.to(torch.bfloat16) if t.is_floating_point() else t)(torch.as_tensor(a)))
    def _A_setup():
        B, L, D, N = 1, 2048, 448, 256
        A_re = -(lambda _x: torch.exp(torch.as_tensor(_x)))((lambda *s: torch.randn(*s, dtype=torch.bfloat16) * 6.4)(D, N))
        A_im = torch.pi * (lambda *s: torch.randn(*s, dtype=torch.bfloat16) * 0.1)(D, N)
        B_re = (lambda *s: torch.randn(*s, dtype=torch.bfloat16) * 0.2)(D, N)
        B_im = (lambda *s: torch.randn(*s, dtype=torch.bfloat16) * 0.2)(D, N)
        C_re = (lambda *s: torch.randn(*s, dtype=torch.bfloat16) * 0.2)(D, N)
        C_im = (lambda *s: torch.randn(*s, dtype=torch.bfloat16) * 0.2)(D, N)
        log_dt = (lambda *s: torch.randn(*s, dtype=torch.bfloat16) * 0.1)(D) * 0.5 - 4.0
        Dskip = (lambda *s: torch.randn(*s, dtype=torch.bfloat16) * 0.1)(D)
        return {
            "u": _w((lambda *s: torch.randn(*s, dtype=torch.bfloat16) * 0.05)(B, L, D)),
            "A_re": _w(A_re),
            "A_im": _w(A_im),
            "B_re": _w(B_re),
            "B_im": _w(B_im),
            "C_re": _w(C_re),
            "C_im": _w(C_im),
            "log_dt": _w(log_dt),
            "Dskip": _w(Dskip),
        }
    _A_in = _A_setup()

    def _B_setup():
        B, S, D, Dff = 1, 2048, 448, 1792
        return {
            "x": _w((lambda *s: torch.randn(*s, dtype=torch.bfloat16) * 0.05)(B, S, D)),
            "w1": _w((lambda *s: torch.randn(*s, dtype=torch.bfloat16) * 0.05)(D, Dff)),
            "w2": _w((lambda *s: torch.randn(*s, dtype=torch.bfloat16) * 0.05)(Dff, D)),
        }
    _B_in = _B_setup()

    return {
        "A_u": _A_in["u"],
        "A_A_re": _A_in["A_re"],
        "A_A_im": _A_in["A_im"],
        "A_B_re": _A_in["B_re"],
        "A_B_im": _A_in["B_im"],
        "A_C_re": _A_in["C_re"],
        "A_C_im": _A_in["C_im"],
        "A_log_dt": _A_in["log_dt"],
        "A_Dskip": _A_in["Dskip"],
        "B_x": _B_in["x"],
        "B_w1": _B_in["w1"],
        "B_w2": _B_in["w2"],
    }

def reference(A_u, A_A_re, A_A_im, A_B_re, A_B_im, A_C_re, A_C_im, A_log_dt, A_Dskip, B_x, B_w1, B_w2):
    def _A_ref(u, A_re, A_im, B_re, B_im, C_re, C_im, log_dt, Dskip):
        Bsz, L, D = u.shape
        dt = torch.exp(log_dt)[:, None]
        A = A_re + 1j * A_im
        Bc = B_re + 1j * B_im
        Cc = C_re + 1j * C_im
        logdA = dt * A
        dB = dt * Bc
        l = (lambda x, dt: x.to(dt))(torch.arange(L), A_re.dtype)
        P = torch.exp(logdA[:, :, None] * l[None, None, :])
        K = torch.einsum("dn,dnl,dn->dl", Cc, P, dB).real
        L2 = 2 * L
        Kp = _pad(K, ((0, 0), (0, L)))
        up = (lambda x, *d: x.permute(*d))(u, 0, 2, 1)
        up = _pad(up, ((0, 0), (0, 0), (0, L)))
        Kf = (lambda x, axes=(-2,-1): torch.fft.rfft2(x.float(), dim=axes))(Kp[:, None, :], axes=(-2, -1))
        Uf = (lambda x, axes=(-2,-1): torch.fft.rfft2(x.float(), dim=axes))(up[:, :, None, :], axes=(-2, -1))
        Yf = Uf * Kf[None, :, :, :]
        y = (lambda x, s, axes=(-2,-1): torch.fft.irfft2(x, s=s, dim=axes).to(torch.bfloat16))(Yf, (1, L2), axes=(-2, -1))[:, :, 0, :L]
        y = (lambda x, *d: x.permute(*d))(y, 0, 2, 1) + u * Dskip[None, None, :]
        return y

    def _B_ref(x, w1, w2):
        h = torch.einsum("bsd,df->bsf", x, w1)
        zero = torch.zeros_like(h)
        h = torch.maximum(h, zero)
        return torch.einsum("bsf,fd->bsd", h, w2)

    x = A_u
    xn = _layernorm(x, axis=-1)
    return x + _A_ref(xn, A_A_re, A_A_im, A_B_re, A_B_im, A_C_re, A_C_im, A_log_dt, A_Dskip) + _B_ref(xn, B_w1, B_w2)
```

```cpp
#include <hip/hip_runtime.h>
#include <hip/hip_bf16.h>
#include <stdint.h>

constexpr int L = 2048, D = 448, N = 256, F = 1792;
typedef unsigned short u16;
__device__ __forceinline__ float bf2f(u16 v) { return __uint_as_float(((unsigned)v) << 16); }
__device__ __forceinline__ u16 f2bf(float f) { unsigned u = __float_as_uint(f); return (u16)((u + 0x7FFF + ((u >> 16) & 1)) >> 16); }
__device__ __forceinline__ float rbf(float f) { return bf2f(f2bf(f)); }

__global__ void ln_k(const u16* x, u16* xn) {
    int row = blockIdx.x; int t = threadIdx.x;
    float v[7]; float s = 0;
    for (int i = 0; i < 7; i++) { v[i] = bf2f(x[row * D + t + 64 * i]); s += v[i]; }
    for (int o = 32; o; o >>= 1) s += __shfl_xor(s, o);
    float m = s / D; float q = 0;
    for (int i = 0; i < 7; i++) { float e = v[i] - m; q += e * e; }
    for (int o = 32; o; o >>= 1) q += __shfl_xor(q, o);
    float var = rbf(q / D); float mb = rbf(m);
    float sd = rbf(sqrtf(rbf(var + 1e-5f)));
    for (int i = 0; i < 7; i++) xn[row * D + t + 64 * i] = f2bf(rbf(v[i] - mb) / sd);
}
__global__ void kgen_k(const u16* Are, const u16* Aim, const u16* Bre, const u16* Bim, const u16* Cre, const u16* Cim,
                       const u16* logdt, float* K) {
    int d = blockIdx.y; int l = blockIdx.x * 256 + threadIdx.x;
    __shared__ float zr[N], zi[N], cr[N], ci[N];
    float dt = rbf(expf(bf2f(logdt[d])));
    { int n = threadIdx.x;
      zr[n] = dt * bf2f(Are[d * N + n]); zi[n] = dt * bf2f(Aim[d * N + n]);
      float br = dt * bf2f(Bre[d * N + n]), bi = dt * bf2f(Bim[d * N + n]);
      float c0 = bf2f(Cre[d * N + n]), c1 = bf2f(Cim[d * N + n]);
      cr[n] = c0 * br - c1 * bi; ci[n] = c0 * bi + c1 * br; }
    __syncthreads();
    float lf = rbf((float)l); float acc = 0;
    for (int n = 0; n < N; n++) {
        float e = expf(zr[n] * lf); float s, c; sincosf(zi[n] * lf, &s, &c);
        acc += e * (cr[n] * c - ci[n] * s);
    }
    K[d * L + l] = acc;
}
__global__ void conv_k(const float* K, const u16* xn, u16* y) {
    int d = blockIdx.y; int t = blockIdx.x * 256 + threadIdx.x;
    __shared__ float sk[L], su[L];
    for (int i = threadIdx.x; i < L; i += 256) { sk[i] = K[d * L + i]; su[i] = bf2f(xn[i * D + d]); }
    __syncthreads();
    float acc = 0;
    for (int j = 0; j <= t; j++) acc += sk[j] * su[t - j];
    y[t * D + d] = f2bf(acc);
}
__global__ void g1_k(const u16* xn, const u16* w1, u16* h) {
    int row = blockIdx.y; int f = blockIdx.x * 256 + threadIdx.x;
    __shared__ float sx[D];
    for (int i = threadIdx.x; i < D; i += 256) sx[i] = bf2f(xn[row * D + i]);
    __syncthreads();
    float acc = 0;
    for (int k = 0; k < D; k++) acc += sx[k] * bf2f(w1[k * F + f]);
    h[row * F + f] = f2bf(fmaxf(rbf(acc), 0.f));
}
__global__ void g2_k(const u16* h, const u16* w2, const u16* x, const u16* xn, const u16* y, const u16* Dsk, u16* out) {
    int row = blockIdx.x; int d = threadIdx.x;
    __shared__ float sh[F];
    for (int i = threadIdx.x; i < F; i += 448) sh[i] = bf2f(h[row * F + i]);
    __syncthreads();
    float acc = 0;
    for (int k = 0; k < F; k++) acc += sh[k] * bf2f(w2[k * D + d]);
    float b = rbf(acc);
    float a = rbf(bf2f(y[row * D + d]) + rbf(bf2f(xn[row * D + d]) * bf2f(Dsk[d])));
    out[row * D + d] = f2bf(rbf(bf2f(x[row * D + d]) + a) + b);
}
extern "C" void kernel_launch(void* const* d_in, const int* in_sizes, int n_in,
                              void* d_out, int out_size, void* d_ws, size_t ws_size,
                              hipStream_t stream) {
    const u16* x = (const u16*)d_in[0];
    char* ws = (char*)d_ws;
    u16* xn = (u16*)ws; u16* y = (u16*)(ws + (4 << 20)); float* K = (float*)(ws + (8 << 20)); u16* h = (u16*)(ws + (16 << 20));
    ln_k<<<L, 64, 0, stream>>>(x, xn);
    kgen_k<<<dim3(L / 256, D), 256, 0, stream>>>((const u16*)d_in[1], (const u16*)d_in[2], (const u16*)d_in[3], (const u16*)d_in[4],
        (const u16*)d_in[5], (const u16*)d_in[6], (const u16*)d_in[7], K);
    conv_k<<<dim3(L / 256, D), 256, 0, stream>>>(K, xn, y);
    g1_k<<<dim3(F / 256, L), 256, 0, stream>>>(xn, (const u16*)d_in[10], h);
    g2_k<<<L, 448, 0, stream>>>(h, (const u16*)d_in[11], x, xn, y, (const u16*)d_in[8], (u16*)d_out);
}
```

```cpp
#include <hip/hip_runtime.h>
#include <hip/hip_bf16.h>
#include <stdint.h>

constexpr int L = 2048, D = 448, N = 256, F = 1792;
typedef unsigned short u16;
__device__ __forceinline__ float bf2f(u16 v) { return __uint_as_float(((unsigned)v) << 16); }
__device__ __forceinline__ u16 f2bf(float f) { unsigned u = __float_as_uint(f); return (u16)((u + 0x7FFF + ((u >> 16) & 1)) >> 16); }
__device__ __forceinline__ float rbf(float f) { return bf2f(f2bf(f)); }

__global__ void ln_k(const u16* x, u16* xn) {
    int row = blockIdx.x; int t = threadIdx.x;
    float v[7]; float s = 0;
    for (int i = 0; i < 7; i++) { v[i] = bf2f(x[row * D + t + 64 * i]); s += v[i]; }
    for (int o = 32; o; o >>= 1) s += __shfl_xor(s, o);
    float m = s / D; float q = 0;
    for (int i = 0; i < 7; i++) { float e = v[i] - m; q += e * e; }
    for (int o = 32; o; o >>= 1) q += __shfl_xor(q, o);
    float var = rbf(q / D); float mb = rbf(m);
    float sd = rbf(sqrtf(rbf(var + 1e-5f)));
    for (int i = 0; i < 7; i++) xn[row * D + t + 64 * i] = f2bf(rbf(v[i] - mb) / sd);
}
__global__ void kgen_k(const u16* Are, const u16* Aim, const u16* Bre, const u16* Bim, const u16* Cre, const u16* Cim,
                       const u16* logdt, float* K) {
    int d = blockIdx.y; int l = blockIdx.x * 256 + threadIdx.x;
    __shared__ float zr[N], zi[N], cr[N], ci[N];
    float dt = rbf(expf(bf2f(logdt[d])));
    { int n = threadIdx.x;
      zr[n] = dt * bf2f(Are[d * N + n]); zi[n] = dt * bf2f(Aim[d * N + n]);
      float br = dt * bf2f(Bre[d * N + n]), bi = dt * bf2f(Bim[d * N + n]);
      float c0 = bf2f(Cre[d * N + n]), c1 = bf2f(Cim[d * N + n]);
      cr[n] = c0 * br - c1 * bi; ci[n] = c0 * bi + c1 * br; }
    __syncthreads();
    float lf = rbf((float)l); float acc = 0;
    for (int n = 0; n < N; n++) {
        float e = expf(zr[n] * lf); float s, c; sincosf(zi[n] * lf, &s, &c);
        acc += e * (cr[n] * c - ci[n] * s);
    }
    K[d * L + l] = acc;
}
__global__ void conv_k(const float* K, const u16* xn, u16* y) {
    int d = blockIdx.y; int t = blockIdx.x * 256 + threadIdx.x;
    __shared__ float sk[L], su[L];
    for (int i = threadIdx.x; i < L; i += 256) { sk[i] = K[d * L + i]; su[i] = bf2f(xn[i * D + d]); }
    __syncthreads();
    float acc = 0;
    for (int j = 0; j <= t; j++) acc += sk[j] * su[t - j];
    y[t * D + d] = f2bf(acc);
}
__global__ void g1_k(const u16* xn, const u16* w1, u16* h) {
    int row = blockIdx.y; int f = blockIdx.x * 256 + threadIdx.x;
    __shared__ float sx[D];
    for (int i = threadIdx.x; i < D; i += 256) sx[i] = bf2f(xn[row * D + i]);
    __syncthreads();
    float acc = 0;
    for (int k = 0; k < D; k++) acc += sx[k] * bf2f(w1[k * F + f]);
    h[row * F + f] = f2bf(fmaxf(rbf(acc), 0.f));
}
__global__ void g2_k(const u16* h, const u16* w2, const u16* x, const u16* xn, const u16* y, const u16* Dsk, u16* out) {
    int row = blockIdx.x; int d = threadIdx.x;
    __shared__ float sh[F];
    for (int i = threadIdx.x; i < F; i += 448) sh[i] = bf2f(h[row * F + i]);
    __syncthreads();
    float acc = 0;
    for (int k = 0; k < F; k++) acc += sh[k] * bf2f(w2[k * D + d]);
    float b = rbf(acc);
    float a = rbf(bf2f(y[row * D + d]) + rbf(bf2f(xn[row * D + d]) * bf2f(Dsk[d])));
    out[row * D + d] = f2bf(rbf(bf2f(x[row * D + d]) + a) + b);
}

typedef unsigned v4u __attribute__((ext_vector_type(4)));
__device__ __forceinline__ unsigned long long rt() { return __builtin_amdgcn_s_memrealtime(); }
template<int MODE>
__global__ __launch_bounds__(256, 1) void probe_k(const char* x, const char* w1, const char* w2, unsigned* sink) {
    extern __shared__ __attribute__((aligned(16))) char smem[];
    int b = blockIdx.x; int tid = threadIdx.x, wid = __builtin_amdgcn_readfirstlane(tid >> 6), lane = tid & 63;
    unsigned long long t0 = rt();
    int xcd = b & 7, i = b >> 3;
    v4u acc = {0,0,0,0};
    if (MODE == 0) {
        int rb = (xcd >> 1) * 4 + i / 7, cb = (xcd & 1) * 7 + i % 7;
        v4u a[28];
        const char* xb = x + (size_t)(rb * 128 + wid * 32) * 896;
#pragma unroll
        for (int s = 0; s < 28; s++) a[s] = *(const v4u*)(xb + s * 1024 + lane * 16);
#pragma unroll
        for (int s = 0; s < 28; s++) {
            int row = s * 16 + wid * 4 + (lane >> 4);
            __builtin_amdgcn_global_load_lds((const unsigned*)(w1 + (size_t)row * 3584 + cb * 256 + (lane & 15) * 16),
                (unsigned*)(smem + (s * 4 + wid) * 1024), 16, 0, 0);
        }
#pragma unroll
        for (int s = 0; s < 28; s++) acc ^= a[s];
        asm volatile("s_waitcnt vmcnt(0)" ::: "memory");
    } else if (MODE == 1) {
        acc = *(const v4u*)(x + b * 4096 + tid * 16);
    } else if (MODE == 2) {
    }
    if (acc.x == 0x12345678 && acc.y == 0x9abcdef0) sink[tid] = acc.z ^ acc.w;
    __syncthreads();
    unsigned long long t1 = rt();
    unsigned long long tgt = t0 + (t1 - t0) * 100;
    while (rt() < tgt) __builtin_amdgcn_s_sleep(2);
}
extern "C" void kernel_launch(void* const* d_in, const int* in_sizes, int n_in,
                              void* d_out, int out_size, void* d_ws, size_t ws_size,
                              hipStream_t stream) {
    const u16* x = (const u16*)d_in[0];
    char* ws = (char*)d_ws;
    u16* xn = (u16*)ws; u16* y = (u16*)(ws + (4 << 20)); float* K = (float*)(ws + (8 << 20)); u16* h = (u16*)(ws + (16 << 20));
    hipFuncSetAttribute((void*)probe_k<2>, hipFuncAttributeMaxDynamicSharedMemorySize, 120 * 1024);
    probe_k<2><<<224, 256, 120*1024, stream>>>((const char*)d_in[0], (const char*)d_in[10], (const char*)d_in[11], (unsigned*)(ws + (64<<20)));
    ln_k<<<L, 64, 0, stream>>>(x, xn);
    kgen_k<<<dim3(L / 256, D), 256, 0, stream>>>((const u16*)d_in[1], (const u16*)d_in[2], (const u16*)d_in[3], (const u16*)d_in[4],
        (const u16*)d_in[5], (const u16*)d_in[6], (const u16*)d_in[7], K);
    conv_k<<<dim3(L / 256, D), 256, 0, stream>>>(K, xn, y);
    g1_k<<<dim3(F / 256, L), 256, 0, stream>>>(xn, (const u16*)d_in[10], h);
    g2_k<<<L, 448, 0, stream>>>(h, (const u16*)d_in[11], x, xn, y, (const u16*)d_in[8], (u16*)d_out);
}
```

```cpp
#include <hip/hip_runtime.h>
#include <hip/hip_bf16.h>
#include <stdint.h>

constexpr int L = 2048, D = 448, N = 256, F = 1792;
typedef unsigned short u16;
__device__ __forceinline__ float bf2f(u16 v) { return __uint_as_float(((unsigned)v) << 16); }
__device__ __forceinline__ u16 f2bf(float f) { unsigned u = __float_as_uint(f); return (u16)((u + 0x7FFF + ((u >> 16) & 1)) >> 16); }
__device__ __forceinline__ float rbf(float f) { return bf2f(f2bf(f)); }

__global__ void ln_k(const u16* x, u16* xn) {
    int row = blockIdx.x; int t = threadIdx.x;
    float v[7]; float s = 0;
    for (int i = 0; i < 7; i++) { v[i] = bf2f(x[row * D + t + 64 * i]); s += v[i]; }
    for (int o = 32; o; o >>= 1) s += __shfl_xor(s, o);
    float m = s / D; float q = 0;
    for (int i = 0; i < 7; i++) { float e = v[i] - m; q += e * e; }
    for (int o = 32; o; o >>= 1) q += __shfl_xor(q, o);
    float var = rbf(q / D); float mb = rbf(m);
    float sd = rbf(sqrtf(rbf(var + 1e-5f)));
    for (int i = 0; i < 7; i++) xn[row * D + t + 64 * i] = f2bf(rbf(v[i] - mb) / sd);
}
__global__ void kgen_k(const u16* Are, const u16* Aim, const u16* Bre, const u16* Bim, const u16* Cre, const u16* Cim,
                       const u16* logdt, float* K) {
    int d = blockIdx.y; int l = blockIdx.x * 256 + threadIdx.x;
    __shared__ float zr[N], zi[N], cr[N], ci[N];
    float dt = rbf(expf(bf2f(logdt[d])));
    { int n = threadIdx.x;
      zr[n] = dt * bf2f(Are[d * N + n]); zi[n] = dt * bf2f(Aim[d * N + n]);
      float br = dt * bf2f(Bre[d * N + n]), bi = dt * bf2f(Bim[d * N + n]);
      float c0 = bf2f(Cre[d * N + n]), c1 = bf2f(Cim[d * N + n]);
      cr[n] = c0 * br - c1 * bi; ci[n] = c0 * bi + c1 * br; }
    __syncthreads();
    float lf = rbf((float)l); float acc = 0;
    for (int n = 0; n < N; n++) {
        float e = expf(zr[n] * lf); float s, c; sincosf(zi[n] * lf, &s, &c);
        acc += e * (cr[n] * c - ci[n] * s);
    }
    K[d * L + l] = acc;
}
__global__ void conv_k(const float* K, const u16* xn, u16* y) {
    int d = blockIdx.y; int t = blockIdx.x * 256 + threadIdx.x;
    __shared__ float sk[L], su[L];
    for (int i = threadIdx.x; i < L; i += 256) { sk[i] = K[d * L + i]; su[i] = bf2f(xn[i * D + d]); }
    __syncthreads();
    float acc = 0;
    for (int j = 0; j <= t; j++) acc += sk[j] * su[t - j];
    y[t * D + d] = f2bf(acc);
}
__global__ void g1_k(const u16* xn, const u16* w1, u16* h) {
    int row = blockIdx.y; int f = blockIdx.x * 256 + threadIdx.x;
    __shared__ float sx[D];
    for (int i = threadIdx.x; i < D; i += 256) sx[i] = bf2f(xn[row * D + i]);
    __syncthreads();
    float acc = 0;
    for (int k = 0; k < D; k++) acc += sx[k] * bf2f(w1[k * F + f]);
    h[row * F + f] = f2bf(fmaxf(rbf(acc), 0.f));
}
__global__ void g2_k(const u16* h, const u16* w2, const u16* x, const u16* xn, const u16* y, const u16* Dsk, u16* out) {
    int row = blockIdx.x; int d = threadIdx.x;
    __shared__ float sh[F];
    for (int i = threadIdx.x; i < F; i += 448) sh[i] = bf2f(h[row * F + i]);
    __syncthreads();
    float acc = 0;
    for (int k = 0; k < F; k++) acc += sh[k] * bf2f(w2[k * D + d]);
    float b = rbf(acc);
    float a = rbf(bf2f(y[row * D + d]) + rbf(bf2f(xn[row * D + d]) * bf2f(Dsk[d])));
    out[row * D + d] = f2bf(rbf(bf2f(x[row * D + d]) + a) + b);
}

__device__ __forceinline__ unsigned long long rt() { return __builtin_amdgcn_s_memrealtime(); }
template<int N>
__global__ __launch_bounds__(256, 1) void probe_k(float* out) {
    unsigned long long t0 = rt();
    float a = threadIdx.x, b = 1.0001f, c = 0.5f;
    float a2 = a + 1, a3 = a + 2, a4 = a + 3;
#pragma unroll
    for (int i = 0; i < N / 4; i++) {
        asm volatile("v_fma_f32 %0, %0, %1, %2" : "+v"(a) : "v"(b), "v"(c));
        asm volatile("v_fma_f32 %0, %0, %1, %2" : "+v"(a2) : "v"(b), "v"(c));
        asm volatile("v_fma_f32 %0, %0, %1, %2" : "+v"(a3) : "v"(b), "v"(c));
        asm volatile("v_fma_f32 %0, %0, %1, %2" : "+v"(a4) : "v"(b), "v"(c));
    }
    unsigned long long t1 = rt();
    if (a + a2 + a3 + a4 == 12345.f) out[threadIdx.x] = a;
    unsigned long long tgt = t0 + (t1 - t0) * 40;
    while (rt() < tgt) __builtin_amdgcn_s_sleep(2);
}
extern "C" void kernel_launch(void* const* d_in, const int* in_sizes, int n_in,
                              void* d_out, int out_size, void* d_ws, size_t ws_size,
                              hipStream_t stream) {
    const u16* x = (const u16*)d_in[0];
    char* ws = (char*)d_ws;
    u16* xn = (u16*)ws; u16* y = (u16*)(ws + (4 << 20)); float* K = (float*)(ws + (8 << 20)); u16* h = (u16*)(ws + (16 << 20));
    probe_k<512><<<256, 256, 0, stream>>>((float*)(ws + (64<<20)));
    ln_k<<<L, 64, 0, stream>>>(x, xn);
    kgen_k<<<dim3(L / 256, D), 256, 0, stream>>>((const u16*)d_in[1], (const u16*)d_in[2], (const u16*)d_in[3], (const u16*)d_in[4],
        (const u16*)d_in[5], (const u16*)d_in[6], (const u16*)d_in[7], K);
    conv_k<<<dim3(L / 256, D), 256, 0, stream>>>(K, xn, y);
    g1_k<<<dim3(F / 256, L), 256, 0, stream>>>(xn, (const u16*)d_in[10], h);
    g2_k<<<L, 448, 0, stream>>>(h, (const u16*)d_in[11], x, xn, y, (const u16*)d_in[8], (u16*)d_out);
}
```

```cpp
#include <hip/hip_runtime.h>
#include <hip/hip_bf16.h>
#include <stdint.h>

constexpr int L = 2048, D = 448, N = 256, F = 1792;
typedef unsigned short u16;
__device__ __forceinline__ float bf2f(u16 v) { return __uint_as_float(((unsigned)v) << 16); }
__device__ __forceinline__ u16 f2bf(float f) { unsigned u = __float_as_uint(f); return (u16)((u + 0x7FFF + ((u >> 16) & 1)) >> 16); }
__device__ __forceinline__ float rbf(float f) { return bf2f(f2bf(f)); }

__global__ void ln_k(const u16* x, u16* xn) {
    int row = blockIdx.x; int t = threadIdx.x;
    float v[7]; float s = 0;
    for (int i = 0; i < 7; i++) { v[i] = bf2f(x[row * D + t + 64 * i]); s += v[i]; }
    for (int o = 32; o; o >>= 1) s += __shfl_xor(s, o);
    float m = s / D; float q = 0;
    for (int i = 0; i < 7; i++) { float e = v[i] - m; q += e * e; }
    for (int o = 32; o; o >>= 1) q += __shfl_xor(q, o);
    float var = rbf(q / D); float mb = rbf(m);
    float sd = rbf(sqrtf(rbf(var + 1e-5f)));
    for (int i = 0; i < 7; i++) xn[row * D + t + 64 * i] = f2bf(rbf(v[i] - mb) / sd);
}
__global__ void kgen_k(const u16* Are, const u16* Aim, const u16* Bre, const u16* Bim, const u16* Cre, const u16* Cim,
                       const u16* logdt, float* K) {
    int d = blockIdx.y; int l = blockIdx.x * 256 + threadIdx.x;
    __shared__ float zr[N], zi[N], cr[N], ci[N];
    float dt = rbf(expf(bf2f(logdt[d])));
    { int n = threadIdx.x;
      zr[n] = dt * bf2f(Are[d * N + n]); zi[n] = dt * bf2f(Aim[d * N + n]);
      float br = dt * bf2f(Bre[d * N + n]), bi = dt * bf2f(Bim[d * N + n]);
      float c0 = bf2f(Cre[d * N + n]), c1 = bf2f(Cim[d * N + n]);
      cr[n] = c0 * br - c1 * bi; ci[n] = c0 * bi + c1 * br; }
    __syncthreads();
    float lf = rbf((float)l); float acc = 0;
    for (int n = 0; n < N; n++) {
        float e = expf(zr[n] * lf); float s, c; sincosf(zi[n] * lf, &s, &c);
        acc += e * (cr[n] * c - ci[n] * s);
    }
    K[d * L + l] = acc;
}
__global__ void conv_k(const float* K, const u16* xn, u16* y) {
    int d = blockIdx.y; int t = blockIdx.x * 256 + threadIdx.x;
    __shared__ float sk[L], su[L];
    for (int i = threadIdx.x; i < L; i += 256) { sk[i] = K[d * L + i]; su[i] = bf2f(xn[i * D + d]); }
    __syncthreads();
    float acc = 0;
    for (int j = 0; j <= t; j++) acc += sk[j] * su[t - j];
    y[t * D + d] = f2bf(acc);
}
__global__ void g1_k(const u16* xn, const u16* w1, u16* h) {
    int row = blockIdx.y; int f = blockIdx.x * 256 + threadIdx.x;
    __shared__ float sx[D];
    for (int i = threadIdx.x; i < D; i += 256) sx[i] = bf2f(xn[row * D + i]);
    __syncthreads();
    float acc = 0;
    for (int k = 0; k < D; k++) acc += sx[k] * bf2f(w1[k * F + f]);
    h[row * F + f] = f2bf(fmaxf(rbf(acc), 0.f));
}
__global__ void g2_k(const u16* h, const u16* w2, const u16* x, const u16* xn, const u16* y, const u16* Dsk, u16* out) {
    int row = blockIdx.x; int d = threadIdx.x;
    __shared__ float sh[F];
    for (int i = threadIdx.x; i < F; i += 448) sh[i] = bf2f(h[row * F + i]);
    __syncthreads();
    float acc = 0;
    for (int k = 0; k < F; k++) acc += sh[k] * bf2f(w2[k * D + d]);
    float b = rbf(acc);
    float a = rbf(bf2f(y[row * D + d]) + rbf(bf2f(xn[row * D + d]) * bf2f(Dsk[d])));
    out[row * D + d] = f2bf(rbf(bf2f(x[row * D + d]) + a) + b);
}

__device__ __forceinline__ unsigned long long rt() { return __builtin_amdgcn_s_memrealtime(); }
template<int N, int PF>
__global__ __launch_bounds__(256, 1) void probe_k(float* out) {
    unsigned long long t0 = rt();
    unsigned pv = 0;
    if (PF) { unsigned long long pc; asm volatile("s_getpc_b64 %0" : "=s"(pc));
        const unsigned* p = (const unsigned*)((pc & ~127ull) + (threadIdx.x & 127) * 128);
        pv = *(const __attribute__((address_space(1))) unsigned*)p;
        __builtin_amdgcn_sched_barrier(0);
        if (PF == 2) { unsigned long long tt = rt(); while (rt() < tt + 120) __builtin_amdgcn_s_sleep(1); t0 = rt(); }
    }
    float a = threadIdx.x, b = 1.0001f, c = 0.5f;
    float a2 = a + 1, a3 = a + 2, a4 = a + 3;
#pragma unroll
    for (int i = 0; i < N / 4; i++) {
        asm volatile("v_fma_f32 %0, %0, %1, %2" : "+v"(a) : "v"(b), "v"(c));
        asm volatile("v_fma_f32 %0, %0, %1, %2" : "+v"(a2) : "v"(b), "v"(c));
        asm volatile("v_fma_f32 %0, %0, %1, %2" : "+v"(a3) : "v"(b), "v"(c));
        asm volatile("v_fma_f32 %0, %0, %1, %2" : "+v"(a4) : "v"(b), "v"(c));
    }
    unsigned long long t1 = rt();
    if (a + a2 + a3 + a4 == 12345.f || pv == 0x1234567) out[threadIdx.x] = a;
    unsigned long long tgt = t0 + (t1 - t0) * 40;
    while (rt() < tgt) __builtin_amdgcn_s_sleep(2);
}
extern "C" void kernel_launch(void* const* d_in, const int* in_sizes, int n_in,
                              void* d_out, int out_size, void* d_ws, size_t ws_size,
                              hipStream_t stream) {
    const u16* x = (const u16*)d_in[0];
    char* ws = (char*)d_ws;
    u16* xn = (u16*)ws; u16* y = (u16*)(ws + (4 << 20)); float* K = (float*)(ws + (8 << 20)); u16* h = (u16*)(ws + (16 << 20));
    probe_k<4096,1><<<256, 256, 0, stream>>>((float*)(ws + (64<<20)));
    ln_k<<<L, 64, 0, stream>>>(x, xn);
    kgen_k<<<dim3(L / 256, D), 256, 0, stream>>>((const u16*)d_in[1], (const u16*)d_in[2], (const u16*)d_in[3], (const u16*)d_in[4],
        (const u16*)d_in[5], (const u16*)d_in[6], (const u16*)d_in[7], K);
    conv_k<<<dim3(L / 256, D), 256, 0, stream>>>(K, xn, y);
    g1_k<<<dim3(F / 256, L), 256, 0, stream>>>(xn, (const u16*)d_in[10], h);
    g2_k<<<L, 448, 0, stream>>>(h, (const u16*)d_in[11], x, xn, y, (const u16*)d_in[8], (u16*)d_out);
}
```

```cpp
#include <hip/hip_runtime.h>
#include <stdint.h>

typedef unsigned long long u64;
typedef unsigned v4u __attribute__((ext_vector_type(4)));
typedef unsigned v2u __attribute__((ext_vector_type(2)));
typedef float f32x2 __attribute__((ext_vector_type(2)));
typedef float f32x4 __attribute__((ext_vector_type(4)));
typedef float f32x16 __attribute__((ext_vector_type(16)));
typedef __bf16 bf16x2 __attribute__((ext_vector_type(2)));
typedef __bf16 bf16x8 __attribute__((ext_vector_type(8)));
typedef _Float16 f16x2 __attribute__((ext_vector_type(2)));
typedef _Float16 f16x8 __attribute__((ext_vector_type(8)));
typedef short s16x4 __attribute__((ext_vector_type(4)));
typedef short s16x2 __attribute__((ext_vector_type(2)));
#define GAS __attribute__((address_space(1)))
#define LAS __attribute__((address_space(3)))
#define RLX_AGENT __ATOMIC_RELAXED, __HIP_MEMORY_SCOPE_AGENT
#define RLX_WG __ATOMIC_RELAXED, __HIP_MEMORY_SCOPE_WORKGROUP
typedef __amdgpu_buffer_rsrc_t rsrc_t;

#define DBG 0
#define WARM 1
#define G2HI 0
#define OUTST 0
#define HWN 12
#define WARM0 3
#define TOGGLE 1
#define GPRIO 2
#define SPRIO 1
#if defined(__HIP_DEVICE_COMPILE__)
#pragma clang attribute push(__attribute__((target("no-packed-fp32-ops"))), apply_to = function)
#endif

constexpr int SEQ = 2048, DM = 448, NMODE = 256, FF = 1792;
constexpr unsigned WS_EPOCH = 0;
constexpr unsigned WS_FLN = 4096;
constexpr unsigned WS_FH = 16384;
constexpr unsigned WS_ST = 3u << 15;
constexpr unsigned WS_XN = 1u << 17;
constexpr unsigned WS_A = WS_XN + (2u << 20);
constexpr unsigned WS_H = WS_A + (2u << 20);
constexpr unsigned WS_END = WS_H + 5u * 112u * 14336u;
__host__ __device__ constexpr unsigned h_tile(unsigned rt) { return (rt / 14) * (112u * 14336u) + (rt % 14) * 1024u; }
constexpr unsigned WS_DBG = 16u << 20;
constexpr int L_GW = 28672;
constexpr int L_S = 0;
constexpr int L_PAIR = 23872, L_RSTR = 4160, L_U = 16640, TROW = 288, L_TAB = 32 * TROW;
constexpr int L_SYNC = L_S + 2 * L_PAIR;
constexpr int SY_DUMP = L_SYNC + 128;
constexpr int L_G = 49152;
constexpr int SY_PG = L_SYNC, SY_PS = L_SYNC + 16, SY_PL = L_SYNC + 32  , SY_CNT = L_SYNC + 64  ,
              SY_E = L_SYNC + 80  ;
constexpr int L_TOTAL = L_G + 4 * L_GW;
static_assert(SY_DUMP + 256 <= L_G, "lds");
static_assert(L_TOTAL <= 163840, "lds");
constexpr unsigned SPIN_G = 400000, SPIN_L = 4000000;

#define WS_BASE(n) (((n) - (20u << 20)) & ~(size_t)0xfffff)
struct Params {
    const uint16_t *x, *are, *aim, *bre, *bim, *cre, *cim, *logdt, *dskip, *w1, *w2;
    uint16_t* out;
    char* ws;
};

template <int V> struct IC { static constexpr int value = V; };
template <int I, int N, class F> __device__ __forceinline__ void sfor(F&& f) { if constexpr (I < N) { f(IC<I>{}); sfor<I + 1, N>(f); } }
__device__ __forceinline__ bool wave_all(bool c) { return __builtin_amdgcn_ballot_w64(!c) == 0; }
__device__ __forceinline__ float asf(unsigned v) { return __builtin_bit_cast(float, v); }
__device__ __forceinline__ unsigned asu(float v) { return __builtin_bit_cast(unsigned, v); }
__device__ __forceinline__ float bflo(unsigned v) { return __builtin_bit_cast(float, v << 16); }
__device__ __forceinline__ float bfhi(unsigned v) { return __builtin_bit_cast(float, v & 0xffff0000u); }
__device__ __forceinline__ unsigned pk(float a, float b) { f32x2 t = {a, b}; return __builtin_bit_cast(unsigned, __builtin_convertvector(t, bf16x2)); }
__device__ __forceinline__ float rbf(float a) { return bflo(pk(a, a)); }
__device__ __forceinline__ float dot2(unsigned a, unsigned b, float c) {
    return __builtin_amdgcn_fdot2_f32_bf16(__builtin_bit_cast(bf16x2, a), __builtin_bit_cast(bf16x2, b), c, false);
}
__device__ __forceinline__ f32x16 mfma32(v4u a, v4u b, f32x16 c) {
    return __builtin_amdgcn_mfma_f32_32x32x16_bf16(__builtin_bit_cast(bf16x8, a), __builtin_bit_cast(bf16x8, b), c, 0, 0, 0);
}
__device__ __forceinline__ f32x4 mfma16(v4u a, v4u b, f32x4 c) {
    return __builtin_amdgcn_mfma_f32_16x16x32_bf16(__builtin_bit_cast(bf16x8, a), __builtin_bit_cast(bf16x8, b), c, 0, 0, 0);
}
__device__ __forceinline__ v4u lds128(unsigned a) { return *(const LAS v4u*)(size_t)a; }
__device__ __forceinline__ v2u lds64(unsigned a) { return *(const LAS v2u*)(size_t)a; }
__device__ __forceinline__ float ldsf(unsigned a) { return *(const LAS float*)(size_t)a; }
__device__ __forceinline__ void sts128(unsigned a, v4u v) { *(LAS v4u*)(size_t)a = v; }
__device__ __forceinline__ void sts64(unsigned a, v2u v) { *(LAS v2u*)(size_t)a = v; }
__device__ __forceinline__ void sts32(unsigned a, unsigned v) { *(LAS unsigned*)(size_t)a = v; }
__device__ __forceinline__ v2u ldtr(unsigned a) { return __builtin_bit_cast(v2u, __builtin_amdgcn_ds_read_tr16_b64_v4i16((LAS s16x4*)(size_t)a)); }
__device__ __forceinline__ v4u ldtr2(unsigned a0, unsigned a1) { v2u l = ldtr(a0), h = ldtr(a1); return v4u{l.x, l.y, h.x, h.y}; }
#define PLO "s_setprio 0\n\t"
#define PHI "\n\ts_setprio %[hi]"
#define PLO2
#define PHI2
__device__ __forceinline__ void set_m0(unsigned v) { asm volatile("s_mov_b32 m0, %0\n\ts_nop 0" ::"s"(v) : "memory"); }
template <int OFF, int HI = 2> __device__ __forceinline__ void dma(rsrc_t rs, unsigned voff, unsigned soff) {
    asm volatile(PLO "buffer_load_dwordx4 %0, %1, %2 offen offset:%3 lds" PHI ::"v"(voff), "s"(rs), "s"(soff), "i"(OFF), [hi] "i"(HI) : "memory");
}
template <int OFF, int HI = 2> __device__ __forceinline__ void dma(rsrc_t rs, unsigned voff, unsigned soff, v4u old0, v4u old1) {
    asm volatile(PLO "buffer_load_dwordx4 %0, %1, %2 offen offset:%3 lds" PHI ::"v"(voff), "s"(rs), "s"(soff), "i"(OFF), "v"(old0), "v"(old1), [hi] "i"(HI) : "memory");
}
template <int OFF, int HI = 2> __device__ __forceinline__ void dma(rsrc_t rs, unsigned voff, unsigned soff, v4u old) {
    asm volatile(PLO "buffer_load_dwordx4 %0, %1, %2 offen offset:%3 lds" PHI ::"v"(voff), "s"(rs), "s"(soff), "i"(OFF), "v"(old), [hi] "i"(HI) : "memory");
}
template <int OFF> __device__ __forceinline__ void ald128(v4u& d, rsrc_t rs, unsigned voff, unsigned soff) {
    asm volatile(PLO2 "buffer_load_dwordx4 %0, %1, %2, %3 offen offset:%4 sc1" PHI2 : "=v"(d) : "v"(voff), "s"(rs), "s"(soff), "i"(OFF) : "memory");
}
template <int OFF> __device__ __forceinline__ void ald64(v2u& d, rsrc_t rs, unsigned voff) {
    asm volatile("buffer_load_dwordx2 %0, %1, %2, 0 offen offset:%3 sc1" : "=v"(d) : "v"(voff), "s"(rs), "i"(OFF) : "memory");
}
__device__ __forceinline__ void pld128(v4u& d, rsrc_t rs, unsigned voff, unsigned soff) {
    asm volatile("buffer_load_dwordx4 %0, %1, %2, %3 offen" : "=v"(d) : "v"(voff), "s"(rs), "s"(soff) : "memory");
}
__device__ __forceinline__ void uld128(v4u& d, rsrc_t rs, unsigned voff) {
    asm volatile("buffer_load_dwordx4 %0, %1, %2, 0 offen sc1" : "=v"(d) : "v"(voff), "s"(rs) : "memory");
}
template <int N, class T> __device__ __forceinline__ void vmwait(T& d) { asm volatile("s_waitcnt vmcnt(%1)" : "+v"(d) : "i"(N) : "memory"); }
__device__ __forceinline__ unsigned pin_s(unsigned v) { asm volatile("" : "+s"(v)); return v; }
__device__ __forceinline__ void m0_only(unsigned v) { asm volatile("s_mov_b32 m0, %0" ::"s"(v) : "memory"); }
template <int O0, int O1, int HI> __device__ __forceinline__ void dma2(unsigned lds, rsrc_t rs, unsigned v0, unsigned v1, unsigned soff, v4u old0, v4u old1) {
    asm volatile("s_mov_b32 m0, %0\n\ts_setprio 0\n\tbuffer_load_dwordx4 %1, %3, %4 offen offset:%5 lds\n\tbuffer_load_dwordx4 %2, %3, %4 offen offset:%6 lds\n\ts_setprio %7"
                 ::"s"(lds), "v"(v0), "v"(v1), "s"(rs), "s"(soff), "i"(O0), "i"(O1), "i"(HI), "v"(old0), "v"(old1) : "memory");
}
template <int O0, int O1, int HI> __device__ __forceinline__ void dma2(unsigned lds, rsrc_t rs, unsigned v0, unsigned v1, unsigned soff) {
    asm volatile("s_mov_b32 m0, %0\n\ts_setprio 0\n\tbuffer_load_dwordx4 %1, %3, %4 offen offset:%5 lds\n\tbuffer_load_dwordx4 %2, %3, %4 offen offset:%6 lds\n\ts_setprio %7"
                 ::"s"(lds), "v"(v0), "v"(v1), "s"(rs), "s"(soff), "i"(O0), "i"(O1), "i"(HI) : "memory");
}
template <int VM, int LG> __device__ __forceinline__ void waitc() {
    static_assert(VM >= 0 && VM <= 63 && LG >= 0 && LG <= 15, "counter range");
    __builtin_amdgcn_s_waitcnt((VM & 15) | ((VM >> 4) << 14) | 0x70 | (LG << 8));
}
__device__ __forceinline__ void st128(v4u v, rsrc_t rs, unsigned voff) { __builtin_amdgcn_raw_buffer_store_b128(v, rs, voff, 0, 16); }
__device__ __forceinline__ v4u ld128(rsrc_t rs, unsigned voff, unsigned soff) { return __builtin_amdgcn_raw_buffer_load_b128(rs, voff, soff, 16); }
template <int N> __device__ __forceinline__ void vmwait() { asm volatile("s_waitcnt vmcnt(%0)" ::"i"(N) : "memory"); }
#define VMWAIT(n) asm volatile("s_waitcnt vmcnt(" #n ")" ::: "memory")
#define LGWAIT() asm volatile("s_waitcnt lgkmcnt(0)" ::: "memory")
#define SB() __builtin_amdgcn_sched_barrier(0)
__device__ __forceinline__ unsigned rfl(unsigned v) { return __builtin_amdgcn_readfirstlane(v); }
__device__ __forceinline__ void prog_set(unsigned addr, unsigned val, int lane) {
    LGWAIT();
    if (lane == 0) __hip_atomic_store((LAS unsigned*)(size_t)addr, val, RLX_WG);
    asm volatile("" ::: "memory");
}
__device__ __forceinline__ void prog_raise(unsigned addr, unsigned val, int lane) {
    if (lane == 0) __hip_atomic_store((LAS unsigned*)(size_t)addr, val, RLX_WG);
    asm volatile("" ::: "memory");
}
__device__ __forceinline__ v4u prog_peek(unsigned addr) { return *(volatile const LAS v4u*)(size_t)addr; }
__device__ __forceinline__ void prog_wait4(unsigned addr, unsigned val) {
    _Pragma("nounroll") for (unsigned s = 0; s < SPIN_L; ++s) {
        v4u p = *(volatile const LAS v4u*)(size_t)addr;
        unsigned m = min(min(p.x, p.y), min(p.z, p.w));
        if (rfl(m) >= val) break;
    }
    asm volatile("" ::: "memory");
}
__device__ __forceinline__ void prog_check4(v4u p, unsigned addr, unsigned val) {
    if (rfl(min(min(p.x, p.y), min(p.z, p.w))) < val) prog_wait4(addr, val);
    asm volatile("" ::: "memory");
}
__device__ __forceinline__ void prog_wait1(unsigned addr, unsigned val) {
    _Pragma("nounroll") for (unsigned s = 0; s < SPIN_L; ++s) {
        unsigned p = *(volatile const LAS unsigned*)(size_t)addr;
        if (rfl(p) >= val) break;
    }
    asm volatile("" ::: "memory");
}
__device__ __forceinline__ u64 get_epoch(v4u e) {
    _Pragma("nounroll") for (unsigned s = 0; s < SPIN_L && rfl(e.z) == 0; ++s) e = *(volatile const LAS v4u*)(size_t)SY_E;
    return ((u64)rfl(e.y) << 32) | rfl(e.x);
}
__device__ __forceinline__ u64 get_epoch() { return get_epoch(*(volatile const LAS v4u*)(size_t)SY_E); }
__device__ __forceinline__ void put_epoch(u64 e, int lane) {
    if (lane == 0) *(volatile LAS v4u*)(size_t)SY_E = v4u{(unsigned)e, (unsigned)(e >> 32), 1u, 0u};
}
__device__ __forceinline__ void poll1(const char* p, u64 e) {
    _Pragma("nounroll") for (unsigned s = 0; s < SPIN_G; ++s) {
        u64 f = __hip_atomic_load((const GAS u64*)p, RLX_AGENT);
        if (wave_all(f == e)) break;
        __builtin_amdgcn_s_sleep(1);
    }
    __builtin_amdgcn_fence(__ATOMIC_ACQUIRE, "wavefront");
}
__device__ __forceinline__ void poll2(const char* p, const char* q, u64 e) {
    _Pragma("nounroll") for (unsigned s = 0; s < SPIN_G; ++s) {
        u64 f = __hip_atomic_load((const GAS u64*)p, RLX_AGENT);
        u64 g = __hip_atomic_load((const GAS u64*)q, RLX_AGENT);
        if (wave_all(f == e && g == e)) break;
        __builtin_amdgcn_s_sleep(1);
    }
    __builtin_amdgcn_fence(__ATOMIC_ACQUIRE, "wavefront");
}
__device__ __forceinline__ void flag_set(char* p, u64 e, int lane) {
    if (lane == 0) __hip_atomic_store((GAS u64*)p, e, RLX_AGENT);
    asm volatile("" ::: "memory");
}
template <int CTRL> __device__ __forceinline__ float dpp_add(float v) {
    return v + __builtin_bit_cast(float, __builtin_amdgcn_mov_dpp(__builtin_bit_cast(int, v), CTRL, 0xf, 0xf, true));
}
__device__ __forceinline__ float sum8(float v) { v = dpp_add<0xB1>(v); v = dpp_add<0x4E>(v); return dpp_add<0x141>(v); }
__device__ __forceinline__ float ln_sd(float var) {
    const float t = rbf(rbf(var) + 1e-5f), s = __builtin_amdgcn_sqrtf(t);
    return rbf(__builtin_fmaf(__builtin_fmaf(-s, s, t), 0.5f * __builtin_amdgcn_rcpf(s), s));
}
__device__ __forceinline__ float rcp_nr(float d) { const float r = __builtin_amdgcn_rcpf(d); return __builtin_fmaf(__builtin_fmaf(-d, r, 1.0f), r, r); }

#define TS(i)
#define TSDECL
#define TSDUMP()
#define TSARG
#define TSPASS

__device__ __forceinline__ void ln_role(const Params& P, rsrc_t rws, int U, int wv, int lane TSARG) {
    u64 e0 = 0;
    if (wv == 0) e0 = __hip_atomic_load((const GAS u64*)(P.ws + WS_EPOCH), RLX_AGENT);
    const int row0 = 8 * wv + (lane >> 3);
    const char* src = (const char*)P.x + (size_t)(64 * U + row0) * 896 + (lane & 7) * 16;
    v4u xr[7];
#pragma unroll
    for (int s = 0; s < 7; s++) xr[s] = *(const GAS v4u*)(src + s * 128);
    SB();
    __builtin_amdgcn_s_barrier();
    asm volatile("" : "+v"(e0));
    if (wv == 0) put_epoch(e0 + 1, lane);
    unsigned ones = 0x3f803f80u;
    asm volatile("" : "+v"(ones));
    float s4[4] = {0.f, 0.f, 0.f, 0.f}, q4[4] = {0.f, 0.f, 0.f, 0.f};
#pragma unroll
    for (int i = 0; i < 28; i++) { const unsigned t = xr[i >> 2][i & 3]; s4[i & 3] = dot2(t, ones, s4[i & 3]); }
    const float mean = sum8((s4[0] + s4[1]) + (s4[2] + s4[3])) * (1.0f / DM), mb = rbf(mean);
    const u64 E = get_epoch();
    const unsigned so = (lane & 7) == 0 ? WS_ST + (64 * U + row0) * 16 : 0x7ffffff0u, eh = (unsigned)(E >> 32) & 0xffffu;
    __builtin_amdgcn_raw_buffer_store_b64(v2u{asu(-mb) | eh, (unsigned)E}, rws, so, 0, 16);
    SB();
    TS(1);
#pragma unroll
    for (int s = 0; s < 7; s++) asm volatile("" : "+v"(xr[s]));
    float v[56];
#pragma unroll
    for (int i = 0; i < 28; i++) { const unsigned t = xr[i >> 2][i & 3]; v[2 * i] = bflo(t) - mb; v[2 * i + 1] = bfhi(t) - mb; }
#pragma unroll
    for (int i = 0; i < 56; i++) q4[i & 3] = __builtin_fmaf(v[i], v[i], q4[i & 3]);
    const float dm = mean - mb;
    const float sd = ln_sd(fmaxf(sum8((q4[0] + q4[1]) + (q4[2] + q4[3])) * (1.0f / DM) - dm * dm, 0.f)), rinv = rcp_nr(sd);
    __builtin_amdgcn_raw_buffer_store_b64(v2u{asu(sd) | eh, (unsigned)E}, rws, so + 8, 0, 16);
    TS(2);
    const unsigned wa = L_G + row0 * 912 + (wv >> 1) * 32 + (lane & 7) * 16;
#pragma unroll
    for (int i = 0; i < 7; i++) {
        v4u o;
#pragma unroll
        for (int k = 0; k < 4; k++) o[k] = pk(v[8 * i + 2 * k] * rinv, v[8 * i + 2 * k + 1] * rinv);
        sts128(wa + i * 128, o);
    }
    TS(6);
    LGWAIT();
    __builtin_amdgcn_s_barrier();
    TS(7);
    if (wv < 7) {
        const int g8 = wv * 8 + (lane >> 3), tb = lane & 7;
        const unsigned ra = L_G + tb * (8 * 912) + (tb >> 1) * 32 + g8 * 16;
        v4u r[8];
#pragma unroll
        for (int i = 0; i < 8; i++) r[i] = lds128(ra + i * 912);
        const unsigned off = WS_XN + g8 * (8 * 4096) + U * 128 + tb * 16;
#pragma unroll
        for (int c = 0; c < 8; c++) {
            const unsigned sel = (c & 1) ? 0x07060302u : 0x05040100u;
            v4u o;
            o.x = __builtin_amdgcn_perm(r[1][c >> 1], r[0][c >> 1], sel); o.y = __builtin_amdgcn_perm(r[3][c >> 1], r[2][c >> 1], sel);
            o.z = __builtin_amdgcn_perm(r[5][c >> 1], r[4][c >> 1], sel); o.w = __builtin_amdgcn_perm(r[7][c >> 1], r[6][c >> 1], sel);
            st128(o, rws, off + c * 4096);
        }
    }
    TS(8);
    const rsrc_t rw2 = __builtin_amdgcn_make_buffer_rsrc((void*)P.w2, 0, DM * FF * 2, 0x00020000);
    const unsigned vV = (lane >> 3) * 896 + (lane & 7) * 16;
    v4u pw[7];
    auto warm = [&](int o) {
#pragma unroll
        for (int m = 0; m < 7; m++) {
            const int idx = (U & 3) * 8 + wv + 32 * m, rr = idx / 7, cb = idx - 7 * rr;
            pld128(pw[m], rw2, vV, (16 * (7 * (rr >> 1) + (4 * cb + WARM0 + o) % 7) + 8 * (rr & 1)) * 896 + cb * 128);
        }
    };
    if (WARM) { warm(0); VMWAIT(7); } else VMWAIT(0);
    __builtin_amdgcn_s_barrier();
    if (wv == 0) flag_set(P.ws + WS_FLN + U * 32, get_epoch(), lane);
    if (WARM) {
#define PWAIT() asm volatile("s_waitcnt vmcnt(0)" : "+v"(pw[0]), "+v"(pw[1]), "+v"(pw[2]), "+v"(pw[3]), "+v"(pw[4]), "+v"(pw[5]), "+v"(pw[6])::"memory")
        PWAIT(); warm(1); PWAIT(); warm(2); PWAIT();
        if (WARM0 == 3) { warm(3); PWAIT(); }
    }
}
constexpr int HW = HWN;
constexpr int G2_FCHK = 28 - HW, G2_FLD = 9;
struct QItem { int place, id; };
constexpr QItem QS[] = {{0, 0}, {0, 10}, {0, 11}, {0, 1}, {0, 12}, {0, 13}, {0, 2}, {0, 14}, {0, 15},
    {100, 3}, {101, 16}, {102, 17}, {103, 4}, {104, 18}, {105, 19}, {106, 5}, {107, 20}, {108, 6}, {109, 21}, {110, 22}, {111, 30}, {112, 23},
    {113, 31}, {114, 70}, {115, 32}, {116, 40}, {119, 33}, {120, 41}, {123, 34}, {124, 42}, {127, 35}, {150, 36}, {150, 72},
    {200, 43}, {201, 71}, {204, 47}, {208, 48}, {212, 74}, {213, 44}, {214, 49}, {215, 45}, {217, 46}, {219, 50}, {221, 51}, {222, 76}, {223, 52}, {225, 53},
    {227, 54}, {250, 73}};
constexpr int QN = sizeof(QS) / sizeof(QS[0]);
constexpr int q_size(int id) { return id == 72 ? 6 : id == 73 ? 4 : (id >= 74 && id <= 77) ? 1 : 2; }
constexpr int g2_step(int J) { return (J + HW < 58 ? 1 : 0) + (J < 13 ? 2 : 0) + (J == 0 ? 1 : 0) + (J == G2_FLD ? 1 : 0) + (J == 48 ? 3 : 0); }
constexpr int q_before(int place) {
    int n = 0;
    for (int k = 0; k < QN; k++) if (QS[k].place < place) n += q_size(QS[k].id);
    if (place >= 300) { n += HW; for (int J = 0; J < place - 300; J++) n += g2_step(J); }
    return n;
}
constexpr int q_pos(int id) {
    if (id >= 100) return id - 100 < HW ? q_before(299) + id - 100 + 1 : q_before(300 + id - 100 - HW) + 1;
    if (id == 77) return q_before(300 + G2_FLD + 1);
    int n = 0;
    for (int k = 0; k < QN; k++) { n += q_size(QS[k].id); if (QS[k].id == id) return n; }
    return -1000;
}
constexpr int q_at(int place);
constexpr int b_rd(int n);
constexpr int q_at(int place) { for (int k = 0; k < QN; k++) if (QS[k].place == place) return QS[k].id; return -1; }
constexpr int g2_v(int J) { return J < 2 ? 61 + J : J < 8 ? 53 + J : 55 + J; }
constexpr int b_rd(int n) { const int nx = q_at(202 + n); return n >= 0 && nx >= 47 && nx < 61 ? nx - 47 : -1; }

__device__ __forceinline__ void gemm_role(const Params& P, int xcd, int i, int w, int lane TSARG) {
    const int ri = i / 7, ci = i % 7;
    const int rb = (xcd >> 1) * 4 + ri, cb = (xcd & 1) * 7 + ci;
    const rsrc_t rx = __builtin_amdgcn_make_buffer_rsrc((char*)P.x - 4096, 0, SEQ * DM * 2 + 4096, 0x00020000);
    const rsrc_t rw1 = __builtin_amdgcn_make_buffer_rsrc((char*)P.w1 - 4096, 0, DM * FF * 2 + 4096, 0x00020000);
    const rsrc_t rw2 = __builtin_amdgcn_make_buffer_rsrc((char*)P.w2 - 4096, 0, DM * FF * 2 + 4096, 0x00020000);
    const rsrc_t rws = __builtin_amdgcn_make_buffer_rsrc((void*)P.ws, 0, WS_END, 0x00020000);
    const int ft = w & 1, par = w >> 1, hh = lane >> 5, r32 = lane & 31;
    const unsigned reg = L_G + w * L_GW, ring = reg + 14336;
    const int c0 = (ci + 2 * ri + (xcd >> 1)) % 7;
    unsigned cw[7], cx[7];
#pragma unroll
    for (int c = 0; c < 7; c++) { int t = c + c0; t = t >= 7 ? t - 7 : t; cw[c] = (128 * cb + 32 * w) * 2 + t * (4 * 57344); cx[c] = (128 * rb + 16 * w) * 896 + t * 128; }
    unsigned vW[4], vX[2];
#pragma unroll
    for (int s = 0; s < 4; s++) vW[s] = 4096 + (lane >> 2) * 3584 + (lane & 3) * 16 + s * (57344 - 1024);
    vX[0] = 4096 + (lane >> 3) * 896 + (((lane & 7) ^ (lane >> 4)) * 16); vX[1] = (vX[0] ^ 64) + 8 * 896 - 1024;
    __builtin_amdgcn_s_barrier();
#pragma unroll
    for (int c = 0; c < 3; c++) {
        dma2<0, 1024, 0>(reg + c * 2048, rx, vX[0], vX[1], cx[c]);
        dma2<0, 1024, 0>(ring + c * 4096, rw1, vW[0], vW[1], cw[c]); dma2<2048, 3072, 0>(ring + c * 4096, rw1, vW[2], vW[3], cw[c]);
    }
    TS(1);
    const int rb2 = 2 * xcd + (ri >> 1), sub = ri & 1, cb2 = ci;
    int rot = 4 * cb2 + 7 * ri; if (rot >= 28) rot -= 28;
    unsigned ks[28];
#pragma unroll
    for (int j = 0; j < 28; j++) { const int t = j + rot; ks[j] = pin_s((28 * w + (t >= 28 ? t - 28 : t)) * 14336); }
    const unsigned q4 = (lane & 15) >> 2, p4 = lane & 3, blk = (lane >> 4) & 1;
    unsigned aW = ring + hh * 512 + q4 * 64 + blk * 32 + p4 * 8;
    unsigned aV = reg + (4 * hh + q4) * 128 + (((2 * blk + (p4 >> 1)) ^ ((q4 >> 1) << 2)) * 16) + (p4 & 1) * 8, aV1 = aV ^ 64;
    unsigned vV0 = 4096 + (lane >> 3) * 896 + (((lane & 7) ^ (((lane >> 4) & 1) << 2)) * 16) + cb2 * 128, vV1 = vV0 + 8 * 896 - 1024;
    unsigned aX[2][4];
#pragma unroll
    for (int s = 0; s < 4; s++) {
        unsigned o = L_G + (r32 >> 4) * L_GW + ((r32 >> 3) & 1) * 1024 + (r32 & 7) * 128 + (((2 * s + hh) ^ ((r32 >> 1) & 7)) * 16);
        aX[0][s] = o; aX[1][s] = o + 2 * L_GW;
        asm volatile("" : "+v"(aX[0][s]), "+v"(aX[1][s]));
    }
    unsigned pgv = lane == 0 ? SY_PG + w * 4 : SY_DUMP + lane * 4, pga = SY_PG;
    unsigned sto = WS_ST + (128 * rb + r32) * 16, hbase = WS_H + (8 * cb + 2 * w) * 14336 + lane * 16;
    v4u wf[28], vf[14][2], xq[3][2], pk4, pe4, ho[4];
    v2u st[2];
    f32x16 acc[2], cs;
    v4u ones4 = {0x3f803f80u, 0x3f803f80u, 0x3f803f80u, 0x3f803f80u};
    asm volatile("" : "+v"(ones4), "+v"(pgv), "+v"(pga), "+v"(aW), "+v"(aV), "+v"(aV1), "+v"(vV0), "+v"(vV1), "+v"(sto), "+v"(hbase));
    unsigned cxb[7], chk, el, eh;
#pragma unroll
    for (int k = 0; k < 7; k++) { cxb[k] = cx[k] + 64 * 896; asm volatile("" : "+s"(cxb[k]), "+s"(cx[k]), "+s"(cw[k])); }
#define XRD(n, T) xq[(n) % 3][T] = lds128(aX[T][(n) & 3] + ((n) >> 2) * 2048)
#define WRD(n) wf[n] = ldtr2(aW + ((n) % 14) * 1024, aW + ((n) % 14) * 1024 + 256)
#define VO(j) ((j) >= 21 ? ((j) - 21) * 2048 : 14336 + ((j) % 7) * 2048)
#define VRD(j, c) ldtr2(((c) ? aV1 : aV) + VO(j), ((c) ? aV1 : aV) + VO(j) + 1024)
#define RAISE(v) do { *(volatile LAS unsigned*)(size_t)pgv = (v); } while (0)
#define CHK1() chk = pin_s(rfl(min(min(pk4.x, pk4.y), min(pk4.z, pk4.w))))
#define CHK2(v) do { if (chk < (v)) prog_wait4(pga, v); asm volatile("" ::: "memory"); } while (0)
#define WAITQ(place, extra, id) vmwait<q_before(place) + (extra) - q_pos(id)>()
    auto issue = [&](auto ii) __attribute__((always_inline)) {
        constexpr int it = decltype(ii)::value;
        if constexpr (it >= 0 && it < 7) dma2<0, 1024, 2>(reg + it * 2048, rx, vX[0], vX[1], cx[it]);
        if constexpr (it >= 10 && it < 24) {
            constexpr int m = 2 * (it - 10), o = (m & 3) * 1024;
            if constexpr (m >= 14) dma2<o, o + 1024, 2>(ring + (m % 14) * 1024 - o, rw1, vW[m & 3], vW[(m & 3) + 1], cw[m >> 2], wf[m - 14], wf[m - 13]);
            else dma2<o, o + 1024, 2>(ring + m * 1024 - o, rw1, vW[m & 3], vW[(m & 3) + 1], cw[m >> 2]);
        }
        if constexpr (it >= 30 && it < 37) dma2<0, 1024, 2>(reg + (it - 30) * 2048, rx, vX[0], vX[1], cxb[it - 30]);
        if constexpr (it >= 40 && it < 68) {
            constexpr int j = it - 40, HI = j >= 15 ? G2HI : 2;
            if constexpr (j < 7) dma2<0, 1024, HI>(reg + VO(j), rw2, vV0, vV1, ks[j], wf[14 + 2 * j], wf[15 + 2 * j]);
            else if constexpr (j < 21) dma2<0, 1024, HI>(reg + VO(j), rw2, vV0, vV1, ks[j], vf[j - 7][0], vf[j - 7][1]);
            else dma2<0, 1024, HI>(reg + VO(j), rw2, vV0, vV1, ks[j]);
        }
        if constexpr (it == 70 || it == 71) { ald64<2 * (it - 70) * 512>(st[0], rws, sto); ald64<(2 * (it - 70) + 1) * 512>(st[1], rws, sto); }
    };
#define PAIR(id) issue(IC<id>{})
    auto means_ok = [&]() { return wave_all((((st[0].x ^ eh) | (st[1].x ^ eh)) & 0xffffu | (st[0].y ^ el) | (st[1].y ^ el)) == 0); };
    auto means_wait = [&](auto hh_) __attribute__((always_inline)) {
        constexpr int H = decltype(hh_)::value;
        _Pragma("nounroll") for (unsigned s = 0; s < SPIN_G && !means_ok(); ++s) {
            ald64<H * 1024>(st[0], rws, sto); ald64<H * 1024 + 512>(st[1], rws, sto);
            vmwait<0>(st[0]); vmwait<0>(st[1]);
        }
    };
    WAITQ(1, 0, 0);
    __builtin_amdgcn_s_setprio(2);
    RAISE(1);
    WAITQ(1, 0, 11);
    TS(2);
    WRD(0); WRD(1);
    prog_wait4(pga, 1);
    XRD(0, 0); XRD(0, 1); XRD(1, 0); XRD(1, 1);
    sfor<0, 28>([&](auto nn) __attribute__((always_inline)) {
        constexpr int n = decltype(nn)::value, c = n >> 2, s = n & 3;
        constexpr int pw = n >= 2 && n < 26 && (n & 1) == 0 ? q_pos(10 + (n + 2) / 2) : 0, px = s == 0 ? q_pos(c < 6 ? c + 1 : 32) : 0;
        constexpr int need = pw > px ? pw : px, prev = n - 1;
        constexpr int lg = n == 0 ? 2 : (prev < 26 ? 4 : 0) + ((prev & 3) == 0 ? 1 : 0) + (prev == 17 ? 1 : 0);
        SB();
        waitc<(need ? q_before(100 + n) - need : 63), lg>();
        SB();
        if constexpr (n == 0) acc[0] = mfma32(wf[n], xq[n % 3][0], f32x16{}); else acc[0] = mfma32(wf[n], xq[n % 3][0], acc[0]);
        SB();
        if constexpr (s == 2) CHK1();
        if constexpr (s == 1) { pk4 = prog_peek(pga); SB(); }
        if constexpr (n < 26) WRD(n + 2);
        if constexpr (n == 19) { const u64 e = get_epoch(pe4); el = pin_s((unsigned)e); eh = pin_s((unsigned)(e >> 32)); }
        SB();
        if constexpr (n == 0) acc[1] = mfma32(wf[n], xq[n % 3][1], f32x16{}); else acc[1] = mfma32(wf[n], xq[n % 3][1], acc[1]);
        SB();
        if constexpr (s == 2) CHK2(c + 2);
        if constexpr (n < 26) { XRD(n + 2, 0); XRD(n + 2, 1); }
        if constexpr (s == 0) RAISE(c + 2);
        if constexpr (n == 17) pe4 = prog_peek(SY_E);
        if constexpr (n == 26) { vmwait<63>(st[0]); vmwait<63>(st[1]); means_wait(IC<0>{}); }
        SB();
        if constexpr (n == 0) cs = mfma32(ones4, wf[n], f32x16{}); else cs = mfma32(ones4, wf[n], cs);
        SB();
        PAIR(q_at(100 + n));
    });
    const u64 E = ((u64)eh << 32) | el;
    XRD(0, 0); XRD(0, 1); XRD(1, 0); XRD(1, 1);
    RAISE(9);
    asm volatile("" : "+v"(acc[0]), "+v"(acc[1]), "+v"(cs));
    SB();
    TS(3);
    v4u sx;
    {
        const float t = cs[0], h0 = rbf(t), r1 = t - h0, h1 = rbf(r1);
        sx = hh ? v4u{0u, 0u, 0u, 0u} : v4u{pk(h0, h1), pk(r1 - h1, 0.f), 0u, 0u};
    }
    auto epi_math = [&]() {
#pragma unroll
        for (int T = 0; T < 2; T++) acc[T] = mfma32(sx, v4u{__builtin_amdgcn_perm(st[T].x, st[T].x, 0x03020302u), st[T].x >> 16, 0u, 0u}, acc[T]);
        pk4 = prog_peek(pga);
#pragma unroll
        for (int T = 0; T < 2; T++)
#pragma unroll
            for (int e = 0; e < 8; e++) {
                s16x2 z = {0, 0};
                ho[2 * T + (e >> 2)][e & 3] = __builtin_bit_cast(unsigned, __builtin_elementwise_max(__builtin_bit_cast(s16x2, pk(acc[T][2 * e], acc[T][2 * e + 1])), z));
                if (T == 1 && e == 0) CHK1();
            }
        asm volatile("" : "+v"(ho[0]), "+v"(ho[1]), "+v"(ho[2]), "+v"(ho[3]));
    };
    auto epi_store = [&](int H) {
#pragma unroll
        for (int k = 0; k < 4; k++) st128(ho[k], rws, hbase + h_tile(4 * rb + 2 * H + (k >> 1)) + (k & 1) * 14336);
        asm volatile("s_nop 1" ::: "memory");
    };
    epi_math();
    CHK2(9);
    PAIR(36);
    epi_store(0);
    {
        const unsigned ao = WS_A + (2 * (xcd * 28 + i) + par) * 4096 + ft * 2048 + lane * 16;
        st128(v4u{~0u, ~0u, ~0u, ~0u}, rws, ao); st128(v4u{~0u, ~0u, ~0u, ~0u}, rws, ao + 1024);
        asm volatile("s_nop 1" ::: "memory");
    }
    TS(4);
    char* fh = P.ws + WS_FH + (xcd * 28 + i) * 128 + w * 8;
    const int trow = 128 * rb2 + 64 * par + 32 * sub, dcol = 64 * cb2 + 32 * ft;
    const int ftile = 14 * w + min(lane, 13), pcb = ftile >> 2;
    const unsigned fpa = WS_FH + (((rb2 >> 2) * 2 + (pcb >= 7)) * 28 + (rb2 & 3) * 7 + pcb - (pcb >= 7 ? 7 : 0)) * 128 + (ftile & 3) * 8;
    unsigned fp0 = hh ? WS_FH + ((dcol + r32) >> 1) * 128 + ((r32 & 1) * 2 + (trow >> 10)) * 8 : fpa;
    asm volatile("" : "+v"(fp0));
    v2u fb;
#define FOK() wave_all(fb.x == el && fb.y == eh)
    sfor<0, 28>([&](auto nn) __attribute__((always_inline)) {
        constexpr int n = decltype(nn)::value, it = q_at(200 + n), rd = b_rd(n), prev = n - 1;
        constexpr int p0 = rd >= 0 ? q_pos(40 + rd) : 0, p1 = n == 6 ? q_pos(34) : n == 14 ? q_pos(36) : it == 74 ? q_pos(72) : n == 24 ? q_pos(71) : 0;
        constexpr int need = p0 > p1 ? p0 : p1;
        constexpr int lg = n == 0 ? 4 : (prev < 26 ? 2 : 0) + (b_rd(prev) >= 0 ? 4 : 0) + (prev == 6 || prev == 14 || prev == 7 || prev == 15 ? 1 : 0);
        SB();
        waitc<(need ? q_before(200 + n) - need : 63), lg>();
        SB();
        if constexpr (n == 0) acc[0] = mfma32(wf[n], xq[n % 3][0], f32x16{}); else acc[0] = mfma32(wf[n], xq[n % 3][0], acc[0]);
        SB();
        if constexpr (n == 10) CHK2(10);
        if constexpr (n == 18) CHK2(11);
        if constexpr (n < 26) { XRD(n + 2, 0); XRD(n + 2, 1); }
        if constexpr (rd >= 0) vf[rd][0] = VRD(rd, 0);
        if constexpr (n == 6) RAISE(10);
        if constexpr (n == 14) RAISE(11);
        if constexpr (n == 7 || n == 15) pk4 = prog_peek(pga);
        SB();
        if constexpr (n == 0) acc[1] = mfma32(wf[n], xq[n % 3][1], f32x16{}); else acc[1] = mfma32(wf[n], xq[n % 3][1], acc[1]);
        SB();
        if constexpr (n == 9 || n == 17) CHK1();
        if constexpr (rd >= 0) vf[rd][1] = VRD(rd, 1);
        if constexpr (it == 74) flag_set(fh, E, lane);
        else if constexpr (it == 76) ald64<0>(fb, rws, fp0);
        else PAIR(it);
        if constexpr (n == 24) { vmwait<63>(st[0]); vmwait<63>(st[1]); means_wait(IC<1>{}); }
    });
    RAISE(12);
    asm volatile("" : "+v"(acc[0]), "+v"(acc[1]));
    SB();
    TS(5);
    __builtin_amdgcn_s_setprio(0);
    vmwait<q_before(250) - q_pos(76)>(fb);
    const bool okA = FOK();
    epi_math();
    epi_store(1);
    TS(6);
    const unsigned aoff = WS_A + (dcol + (lane >> 2)) * 4096 + trow * 2 + (lane & 3) * 16;
    const unsigned xo = (trow + (lane >> 2)) * 896 + dcol * 2 + (lane & 3) * 16;
    const unsigned sx1 = ring + (lane >> 2) * 80 + (lane & 3) * 16, sx2 = ring + r32 * 80 + hh * 8;
    if (!okA) poll1(P.ws + fp0, E);
    TS(7);
    CHK2(12);
    TS(8);
    f32x16 o[2][2];
    const unsigned hv0 = WS_H + h_tile(4 * rb2 + sub) + lane * 16, hv1 = WS_H + h_tile(4 * rb2 + 2 + sub) + lane * 16, sro = WS_ST + (trow + r32) * 16 + 8;
    v4u hr[HW], vq[2][2], a0, a1, xw[2];
    v2u xr[4], sr;
#define HLD(nx) ald128<0>(hr[(nx) % HW], rws, (nx) < 28 ? hv0 : hv1, ks[(nx) % 28])
    sfor<0, HW>([&](auto jj) __attribute__((always_inline)) { HLD(decltype(jj)::value); });
    sfor<0, 56>([&](auto jj) __attribute__((always_inline)) {
        constexpr int J = decltype(jj)::value, H = J / 28, k = J % 28, nx = J + HW, k2 = (J + 2) % 28, k1 = (J + 1) % 28;
        constexpr int p0 = q_pos(100 + J), p1 = J == G2_FCHK ? q_pos(77) : J >= 13 && J <= 18 ? q_before(300 + J - 11) + 3 : 0;
        SB();
        waitc<q_before(300 + J) - (p0 > p1 ? p0 : p1), (k < 14 ? 15 : k1 >= 14 && J < 55 ? (J == 54 ? 6 : 4) : (J == 55 ? 4 : 0))>();
        SB();
        const v4u cur = hr[J % HW];
        v4u b0, b1;
        if constexpr (k < 14) { b0 = vf[k][0]; b1 = vf[k][1]; } else { b0 = vq[J & 1][0]; b1 = vq[J & 1][1]; }
        if constexpr (k == 0) o[H][0] = mfma32(b0, cur, f32x16{}); else o[H][0] = mfma32(b0, cur, o[H][0]);
        SB();
        if constexpr (J < 6) { vf[8 + J][0] = VRD(8 + J, 0); vf[8 + J][1] = VRD(8 + J, 1); }
        if constexpr (k2 >= 14 && J < 54) vq[J & 1][0] = VRD(k2, 0);
        if constexpr (J == G2_FCHK) {
            TS(9);
            vmwait<63>(fb);
            if (!FOK()) poll1(P.ws + fpa + 224 * 128, E);
            TS(10);
        }
        SB();
        if constexpr (k == 0) o[H][1] = mfma32(b1, cur, f32x16{}); else o[H][1] = mfma32(b1, cur, o[H][1]);
        SB();
        if constexpr (k2 >= 14 && J < 54) vq[J & 1][1] = VRD(k2, 1);
        if constexpr (nx < 56) HLD(nx);
        if constexpr (J < 13) PAIR(g2_v(J));
        if constexpr (J == 0) flag_set(fh + 224 * 128, E, lane);
        if constexpr (J == G2_FLD) ald64<0>(fb, rws, fpa + 224 * 128);
        if constexpr (nx == 56 || nx == 57) ald128<0>(xw[nx - 56], rx, xo, 4096 + (nx - 56) * 16 * 896);
        if constexpr (J == 53) { vmwait<4>(xw[0]); sts128(sx1, xw[0]); vmwait<3>(xw[1]); sts128(sx1 + 1280, xw[1]); }
        if constexpr (J == 54) {
#pragma unroll
            for (int j = 0; j < 4; j++) xr[j] = lds64(sx2 + j * 16);
        }
        if constexpr (J == 48) { ald64<0>(sr, rws, sro); ald128<0>(a0, rws, aoff, 0); ald128<0>(a1, rws, aoff, 16 * 4096); }
    });
    SB();
    asm volatile("s_waitcnt vmcnt(0)" : "+v"(a0), "+v"(a1), "+v"(sr)::"memory");
    const bool early = wave_all(max(max(max(a0.x, a0.y), max(a0.z, a0.w)), max(max(a1.x, a1.y), max(a1.z, a1.w))) != 0xffffffffu &&
                                ((sr.x ^ eh) & 0xffffu | (sr.y ^ el)) == 0);
    asm volatile("" : "+v"(o[0][0]), "+v"(o[0][1]), "+v"(o[1][0]), "+v"(o[1][1]));
    TS(11);
    f32x16 fin;
#pragma unroll
    for (int qd = 0; qd < 4; qd++) {
        const f32x16 t = o[qd >> 1][qd & 1];
        if (qd == w) fin = t;
        else {
#pragma unroll
            for (int j = 0; j < 4; j++)
                sts128(reg + ((qd - w - 1) & 3) * 4096 + j * 1024 + lane * 16, __builtin_bit_cast(v4u, f32x4{t[4 * j], t[4 * j + 1], t[4 * j + 2], t[4 * j + 3]}));
        }
    }
    RAISE(13);
    if (!early) {
        _Pragma("nounroll") for (unsigned s = 0; s < SPIN_G; ++s) {
            ald128<0>(a0, rws, aoff, 0); ald128<0>(a1, rws, aoff, 16 * 4096); ald64<0>(sr, rws, sro);
            asm volatile("s_waitcnt vmcnt(0)" : "+v"(a0), "+v"(a1), "+v"(sr)::"memory");
            const unsigned m = max(max(max(a0.x, a0.y), max(a0.z, a0.w)), max(max(a1.x, a1.y), max(a1.z, a1.w)));
            if (wave_all(m != 0xffffffffu && ((sr.x ^ eh) & 0xffffu | (sr.y ^ el)) == 0)) break;
            __builtin_amdgcn_s_sleep(1);
        }
    }
    sts128(ring + 4096 + lane * 16, a0); sts128(ring + 5120 + lane * 16, a1);
    const unsigned aA = ring + 4096 + (4 * hh + q4) * 64 + blk * 32 + p4 * 8;
    v2u av[4];
#pragma unroll
    for (int j = 0; j < 4; j++) av[j] = ldtr(aA + j * 512);
    pk4 = prog_peek(pga);
    TS(12);
    prog_check4(pk4, pga, 13);
    f32x4 pt[3][4];
#pragma unroll
    for (int k = 1; k < 4; k++)
#pragma unroll
        for (int j = 0; j < 4; j++) pt[k - 1][j] = __builtin_bit_cast(f32x4, lds128(L_G + ((w + k) & 3) * L_GW + (3 - k) * 4096 + lane * 16 + j * 1024));
    SB();
    float xa[16];
#pragma unroll
    for (int j = 0; j < 4; j++) {
        xa[4 * j] = bflo(xr[j].x) + bflo(av[j].x); xa[4 * j + 1] = bfhi(xr[j].x) + bfhi(av[j].x);
        xa[4 * j + 2] = bflo(xr[j].y) + bflo(av[j].y); xa[4 * j + 3] = bfhi(xr[j].y) + bfhi(av[j].y);
    }
    const float rinv = rcp_nr(bfhi(sr.x));
    SB();
#pragma unroll
    for (int j = 0; j < 4; j++) {
        float r[4];
#pragma unroll
        for (int e = 0; e < 4; e++) r[e] = __builtin_fmaf((fin[4 * j + e] + pt[0][j][e]) + (pt[1][j][e] + pt[2][j][e]), rinv, xa[4 * j + e]);
        sts64(sx2 + j * 16, v2u{pk(r[0], r[1]), pk(r[2], r[3])});
    }
    const rsrc_t ro = __builtin_amdgcn_make_buffer_rsrc((void*)P.out, 0, SEQ * DM * 2, 0x00020000);
    __builtin_amdgcn_raw_buffer_store_b128(lds128(sx1), ro, xo, 0, OUTST);
    __builtin_amdgcn_raw_buffer_store_b128(lds128(sx1 + 1280), ro, xo + 16 * 896, 0, OUTST);
    TS(13);
}

__device__ __forceinline__ f32x2 cmul(f32x2 a, f32x2 b) { return f32x2{__builtin_fmaf(a.x, b.x, -a.y * b.y), __builtin_fmaf(a.x, b.y, a.y * b.x)}; }
__device__ __forceinline__ f32x2 cexp(float lr, float rev) {
    float e = __builtin_amdgcn_exp2f(lr), f = __builtin_amdgcn_fractf(rev);
    return f32x2{e * __builtin_amdgcn_cosf(f), e * __builtin_amdgcn_sinf(f)};
}
__device__ __forceinline__ f32x2 csqr(f32x2 a) { float t = a.x * a.y; return f32x2{__builtin_fmaf(a.x, a.x, -a.y * a.y), t + t}; }
__device__ __forceinline__ f32x2 cmulc(f32x2 a, f32x2 b) {
    return f32x2{__builtin_fmaf(a.x, b.x, a.y * b.y), __builtin_fmaf(a.y, b.x, -a.x * b.y)};
}
__device__ __forceinline__ unsigned toh(f32x2 a) { return __builtin_bit_cast(unsigned, __builtin_convertvector(a, f16x2)); }
__device__ __forceinline__ void hmul4(unsigned& r0, unsigned& r1, unsigned& r2, unsigned& r3, unsigned a0, unsigned b0, unsigned a1, unsigned b1,
                                      unsigned a2, unsigned b2, unsigned a3, unsigned b3) {
    asm("v_pk_mul_f16 %0, %4, %5 op_sel_hi:[0,1]\n\tv_pk_mul_f16 %1, %6, %7 op_sel_hi:[0,1]\n\t"
        "v_pk_mul_f16 %2, %8, %9 op_sel_hi:[0,1]\n\tv_pk_mul_f16 %3, %10, %11 op_sel_hi:[0,1]\n\t"
        "v_pk_fma_f16 %0, %4, %5, %0 op_sel:[1,1,0] op_sel_hi:[1,0,1] neg_lo:[0,1,0]\n\tv_pk_fma_f16 %1, %6, %7, %1 op_sel:[1,1,0] op_sel_hi:[1,0,1] neg_lo:[0,1,0]\n\t"
        "v_pk_fma_f16 %2, %8, %9, %2 op_sel:[1,1,0] op_sel_hi:[1,0,1] neg_lo:[0,1,0]\n\tv_pk_fma_f16 %3, %10, %11, %3 op_sel:[1,1,0] op_sel_hi:[1,0,1] neg_lo:[0,1,0]"
        : "=&v"(r0), "=&v"(r1), "=&v"(r2), "=&v"(r3) : "v"(a0), "v"(b0), "v"(a1), "v"(b1), "v"(a2), "v"(b2), "v"(a3), "v"(b3));
}
__device__ __forceinline__ void hmulc4(unsigned& r0, unsigned& r1, unsigned& r2, unsigned& r3, unsigned a0, unsigned b0, unsigned a1, unsigned b1,
                                       unsigned a2, unsigned b2, unsigned a3, unsigned b3) {
    asm("v_pk_mul_f16 %0, %4, %5 op_sel_hi:[0,1] neg_hi:[0,1]\n\tv_pk_mul_f16 %1, %6, %7 op_sel_hi:[0,1] neg_hi:[0,1]\n\t"
        "v_pk_mul_f16 %2, %8, %9 op_sel_hi:[0,1] neg_hi:[0,1]\n\tv_pk_mul_f16 %3, %10, %11 op_sel_hi:[0,1] neg_hi:[0,1]\n\t"
        "v_pk_fma_f16 %0, %4, %5, %0 op_sel:[1,1,0] op_sel_hi:[1,0,1]\n\tv_pk_fma_f16 %1, %6, %7, %1 op_sel:[1,1,0] op_sel_hi:[1,0,1]\n\t"
        "v_pk_fma_f16 %2, %8, %9, %2 op_sel:[1,1,0] op_sel_hi:[1,0,1]\n\tv_pk_fma_f16 %3, %10, %11, %3 op_sel:[1,1,0] op_sel_hi:[1,0,1]"
        : "=&v"(r0), "=&v"(r1), "=&v"(r2), "=&v"(r3) : "v"(a0), "v"(b0), "v"(a1), "v"(b1), "v"(a2), "v"(b2), "v"(a3), "v"(b3));
}
template <int ROW> __device__ __forceinline__ void tput(unsigned v) {
    asm volatile("ds_write_addtid_b32 %0 offset:%1" ::"v"(v), "i"(ROW * TROW) : "memory");
}
__device__ __forceinline__ f32x4 mfma16h(v4u a, v4u b, f32x4 c) {
    return __builtin_amdgcn_mfma_f32_16x16x32_f16(__builtin_bit_cast(f16x8, a), __builtin_bit_cast(f16x8, b), c, 0, 0, 0);
}
template <int CTRL> __device__ __forceinline__ unsigned dpp_max(unsigned v) { return max(v, (unsigned)__builtin_amdgcn_mov_dpp((int)v, CTRL, 0xf, 0xf, true)); }
__device__ __forceinline__ void ssm_role(const Params& P, int xcd, int i, int sw, int lane TSARG) {
    const rsrc_t rws = __builtin_amdgcn_make_buffer_rsrc((void*)P.ws, 0, WS_END, 0x00020000);
    const int p = xcd * 28 + i, ch = sw >> 1, mh = sw & 1, d = 2 * p + ch;
    unsigned ldt2, dsk2;
    asm volatile("s_load_dword %0, %2, %4\n\ts_load_dword %1, %3, %4" : "=&s"(ldt2), "=&s"(dsk2) : "s"(P.logdt), "s"(P.dskip), "s"(4 * p) : "memory");
    const unsigned po = d * (NMODE * 2) + mh * 256 + (lane & 15) * 16;
    const int ar = lane >> 4;
    const u64 b01 = (u64)P.are + (ar & 1 ? (u64)P.aim - (u64)P.are : 0), b23 = (u64)P.bre + (ar & 1 ? (u64)P.bim - (u64)P.bre : 0);
    const v4u pa0 = __builtin_nontemporal_load((const GAS v4u*)((ar & 2 ? b23 : b01) + po));
    const v4u pa1 = __builtin_nontemporal_load((const GAS v4u*)((u64)P.cre + (ar & 1 ? (u64)P.cim - (u64)P.cre : 0) + po));
    const v2u ev = __builtin_amdgcn_raw_buffer_load_b64(rws, sw == 0 ? WS_EPOCH : 0x7ffffff0u, 0, 16);
    u64 e0 = ((u64)ev.y << 32) | ev.x;
    SB();
    __builtin_amdgcn_s_barrier();
    __builtin_amdgcn_s_setprio(SPRIO);
    TS(1);
    const unsigned aout = WS_A + d * 4096 + mh * 2048 + lane * 16;
    const unsigned pair = L_S + ch * L_PAIR, kc = pair + 2 * L_TAB;
    const int lam = mh * 64 + lane;
    unsigned gi[19];
#pragma unroll
    for (int k = 0; k < 19; k++) {
        const int tau = 2047 - 16 * lam - k;
        const int ix = tau < 128 ? max(tau, 0) : (int)(pk((float)tau, 0.f) & 0xffffu) - (0x4300 - 128);
        gi[k] = kc + 8 * (ix + (ix >> 5));
        asm volatile("" : "+v"(gi[k]));
    }
    const unsigned tab = pair + mh * L_TAB;
    const int b16 = lane & 15, q = lane >> 4;
    const unsigned tq = tab + q * 16;
    auto setrow = [&](int k, int ix) { return tq + (ix == 0 ? ((k & 3) == 3 ? 28 : 0) : 2 * k + (ix == 1 ? 1 : ix == 2 ? 3 : 2)) * TROW; };
    unsigned aF[7], aC[6];
#pragma unroll
    for (int k = 0; k < 4; k++) { aF[k] = setrow(k, b16 & 3); aC[k] = setrow(k + 2, b16 >> 2); }
    aF[4] = setrow(4, b16 & 3); aF[5] = setrow(b16 < 8 ? 5 : 6, b16 & 3); aF[6] = setrow(7, b16 & 3);
    aC[4] = tq + (18 + (b16 >> 2)) * TROW; aC[5] = tq + (b16 < 8 ? 26 + (b16 >> 2) : 29) * TROW;
    f32x4 k0 = {0.f, 0.f, 0.f, 0.f}, k1 = k0, k2 = k0, k3 = k0;
    asm volatile("" : "+v"(aF[0]), "+v"(aF[1]), "+v"(aF[2]), "+v"(aF[3]), "+v"(aF[4]), "+v"(aF[5]), "+v"(aF[6]), "+v"(aC[0]), "+v"(aC[1]), "+v"(aC[2]), "+v"(aC[3]), "+v"(aC[4]), "+v"(aC[5]));
    set_m0(tab);
    tput<0>(0x00003c00u); tput<28>(0x00003c00u);
    asm volatile("s_waitcnt lgkmcnt(0)" : "+s"(ldt2), "+s"(dsk2)::"memory");
    float dt = rbf(__expf(ch ? bfhi(ldt2) : bflo(ldt2)));
    unsigned pl = tab + 2 * TROW + lane * 4;
    asm volatile("" : "+v"(dt), "+v"(pl));
    sts128(tab + 2 * TROW + lane * 16, pa0); sts128(tab + 2 * TROW + 1024 + lane * 16, pa1);
    struct { unsigned pr[6]; } pre;
#pragma unroll
    for (int j = 0; j < 6; j++) pre.pr[j] = *(const LAS unsigned*)(size_t)(pl + j * 256);
    if (sw == 0) put_epoch(e0 + 1, lane);
    TS(2);
    f32x2 cc0 = cmul(f32x2{bflo(pre.pr[4]), -bflo(pre.pr[5])}, f32x2{dt * bflo(pre.pr[2]), -dt * bflo(pre.pr[3])});
    f32x2 cc1 = cmul(f32x2{bfhi(pre.pr[4]), -bfhi(pre.pr[5])}, f32x2{dt * bfhi(pre.pr[2]), -dt * bfhi(pre.pr[3])});
    float unscale;
    {
        unsigned m = __builtin_bit_cast(unsigned, fmaxf(fmaxf(fabsf(cc0.x), fabsf(cc0.y)), fmaxf(fabsf(cc1.x), fabsf(cc1.y))));
        m = dpp_max<0xB1>(m); m = dpp_max<0x4E>(m); m = dpp_max<0x141>(m); m = dpp_max<0x140>(m);
        const unsigned mm = max(max((unsigned)__builtin_amdgcn_readlane(m, 0), (unsigned)__builtin_amdgcn_readlane(m, 16)),
                                max((unsigned)__builtin_amdgcn_readlane(m, 32), (unsigned)__builtin_amdgcn_readlane(m, 48)));
        const unsigned e = max(mm >> 23, 12u);
        const float sc = __builtin_bit_cast(float, (265u - e) << 23);
        unscale = __builtin_bit_cast(float, (e - 11u) << 23);
        cc0 = f32x2{cc0.x * sc, cc0.y * sc}; cc1 = f32x2{cc1.x * sc, cc1.y * sc};
    }
    v2u fl0;
    const unsigned flo = WS_FLN + (lane & 31) * 32;
    TS(12);
#pragma unroll 1
    for (int pass = 0; pass < 2; pass++) {
        const float lr = dt * (pass ? bfhi(pre.pr[0]) : bflo(pre.pr[0])) * 1.44269504088896341f;
        const float rev = dt * (pass ? bfhi(pre.pr[1]) : bflo(pre.pr[1])) * 0.159154943091895336f;
        const f32x2 cc = pass ? cc1 : cc0;
        f32x2 p[12];
        p[0] = cexp(lr, rev); p[4] = cexp(16.f * lr, 16.f * rev); p[7] = cexp(128.f * lr, 128.f * rev);
        p[1] = csqr(p[0]); p[5] = csqr(p[4]); p[8] = csqr(p[7]); p[2] = csqr(p[1]); p[6] = csqr(p[5]); p[9] = csqr(p[8]);
        p[3] = csqr(p[2]); p[10] = csqr(p[9]); p[11] = csqr(p[10]);
        unsigned hp[10], ht[8];
#pragma unroll
        for (int k = 0; k < 10; k++) hp[k] = toh(p[k]);
        hmul4(ht[0], ht[1], ht[2], ht[3], hp[0], hp[1], hp[1], hp[2], hp[2], hp[3], hp[3], hp[4]);
        hmul4(ht[4], ht[5], ht[6], ht[7], hp[4], hp[5], hp[5], hp[6], hp[6], hp[7], hp[7], hp[8]);
        unsigned cg[11];
        cg[0] = toh(cc); cg[1] = toh(cmulc(cc, p[6])); cg[2] = toh(cmulc(cc, p[7])); cg[4] = toh(cmulc(cc, p[8]));
        cg[6] = toh(cmulc(cc, p[9])); cg[8] = toh(cmulc(cc, p[10])); cg[10] = toh(cmulc(cc, p[11]));
        hmulc4(cg[3], cg[5], cg[7], cg[9], cg[2], hp[6], cg[4], hp[7], cg[6], hp[8], cg[8], hp[9]);
        SB();
        tput<1>(hp[0]); tput<3>(hp[1]); tput<2>(ht[0]); tput<5>(hp[2]); tput<7>(hp[3]); tput<6>(ht[2]); tput<4>(ht[1]); tput<9>(hp[4]); tput<8>(ht[3]);
        tput<11>(hp[5]); tput<10>(ht[4]); tput<18>(cg[0]); tput<19>(cg[1]); tput<20>(cg[2]); tput<21>(cg[3]); tput<13>(hp[6]); tput<12>(ht[5]);
        tput<15>(hp[7]); tput<14>(ht[6]); tput<22>(cg[4]); tput<23>(cg[5]); tput<24>(cg[6]); tput<25>(cg[7]); tput<17>(hp[8]); tput<16>(ht[7]);
        tput<26>(cg[8]); tput<27>(cg[9]); tput<29>(cg[10]);
        v4u ra[4], rb[4], f[7];
#define ULOAD(u) do { constexpr int m_ = (u) % 7, t_ = m_ == 0 ? 0 : m_ == 1 ? 4 : m_ == 2 ? 1 : m_ == 3 ? 2 : m_ == 4 ? 5 : m_ == 5 ? 3 : 6; \
                      ra[(u) & 3] = lds128((t_ == 5 ? aC[4] + 4 * TROW : t_ == 6 ? aC[5] : aC[t_ < 5 ? t_ : 0]) + ((u) / 7) * 64); rb[(u) & 3] = lds128(aF[t_] + ((u) / 7) * 64); } while (0)
        ULOAD(0); ULOAD(1); ULOAD(2);
        sfor<0, 28>([&](auto uu) __attribute__((always_inline)) {
            constexpr int u = decltype(uu)::value, m = u % 7;
            if constexpr (u < 25) ULOAD(u + 3);
            SB();
            const v4u x = ra[u & 3], y = rb[u & 3];
            unsigned r0, r1_, r2_, r3_;
            if constexpr (m == 1 || m == 4 || m == 6) hmulc4(r0, r1_, r2_, r3_, x.x, y.x, x.y, y.y, x.z, y.z, x.w, y.w);
            else hmul4(r0, r1_, r2_, r3_, x.x, y.x, x.y, y.y, x.z, y.z, x.w, y.w);
            f[m] = v4u{r0, r1_, r2_, r3_};
            if constexpr (m == 2) k0 = mfma16h(f[1], f[0], k0);
            if constexpr (u == 14) ald64<0>(fl0, rws, pass ? flo : 0x7ffffff0u);
            if constexpr (u == 26) SB();
            if constexpr (m == 5) { k1 = mfma16h(f[4], f[2], k1); k2 = mfma16h(f[4], f[3], k2); }
            if constexpr (m == 0 && u > 0) k3 = mfma16h(f[6], f[5], k3);
            SB();
        });
        asm volatile("s_nop 1" : "+v"(f[6]));
        k3 = mfma16h(f[6], f[5], k3);
        vmwait<0>(fl0);
        if (pass == 0) TS(3);
    }
    TS(4);
    const u64 E = get_epoch();
    if (!wave_all(fl0.x == (unsigned)E && fl0.y == (unsigned)(E >> 32))) poll1(P.ws + flo, E);
    const unsigned uo = WS_XN + d * 4096 + mh * 2048 + lane * 16;
    v4u u0, u1;
    uld128(u0, rws, uo); uld128(u1, rws, uo + 1024);
    TS(5);
    {
        const unsigned ka = kc + mh * 4 + (66 * q + b16) * 8;
#pragma unroll
        for (int j = 0; j < 4; j++) {
            sts32(ka + (16 * j + (j >> 1)) * 8, asu(k0[j] * unscale));
            sts32(ka + (264 + 16 * j + (j >> 1)) * 8, asu((q < 2 ? k1[j] : k2[j]) * unscale));
        }
        if (q < 3) sts32(ka + 528 * 8, asu(k3[0] * unscale));
        if (q < 2) {
#pragma unroll
            for (int j = 1; j < 4; j++) sts32(ka + (528 + 16 * j + (j >> 1)) * 8, asu(k3[j] * unscale));
        }
    }
    const unsigned me = SY_PS + sw * 4, other = SY_PS + (sw ^ 1) * 4;
    prog_raise(me, 1, lane);
    TS(10);
    prog_wait1(other, 1);
    float rv[19];
#pragma unroll
    for (int k = 0; k < 19; k++) { const v2u t = lds64(gi[k]); rv[k] = asf(t.x) + asf(t.y); }
    prog_raise(me, 2, lane);
    TS(11);
    {
        if (lam == 127) { rv[16] = 0.f; rv[17] = 0.f; rv[18] = 0.f; }
        unsigned ev[9], od[9];
#pragma unroll
        for (int k = 0; k < 9; k++) { ev[k] = pk(rv[2 * k], rv[2 * k + 1]); od[k] = pk(rv[2 * k + 1], rv[2 * k + 2]); }
        const unsigned ra = pair + lam * 32;
        sts128(ra, v4u{ev[0], ev[1], ev[2], ev[3]}); sts128(ra + 16, v4u{ev[4], ev[5], ev[6], ev[7]});
        sts128(ra + L_RSTR, v4u{od[0], od[1], od[2], od[3]}); sts128(ra + L_RSTR + 16, v4u{od[4], od[5], od[6], od[7]});
        sts128(ra + 2 * L_RSTR, v4u{ev[1], ev[2], ev[3], ev[4]}); sts128(ra + 2 * L_RSTR + 16, v4u{ev[5], ev[6], ev[7], ev[8]});
        sts128(ra + 3 * L_RSTR, v4u{od[1], od[2], od[3], od[4]}); sts128(ra + 3 * L_RSTR + 16, v4u{od[5], od[6], od[7], od[8]});
        if (mh == 0 && lane < 40) sts128(lane < 8 ? pair + (lane >> 1) * L_RSTR + 4096 + (lane & 1) * 16 : pair + L_U + (lane - 8) * 16, v4u{0u, 0u, 0u, 0u});
    }
    prog_wait1(other, 2);
    vmwait<0>(u0); vmwait<0>(u1);
    sts128(pair + L_U + 512 + mh * 2048 + lane * 16, u0); sts128(pair + L_U + 512 + mh * 2048 + 1024 + lane * 16, u1);
    prog_raise(me, 3, lane);
    TS(6);
    prog_wait1(other, 3);
    TS(7);
    f32x4 y[8];
#pragma unroll
    for (int j = 0; j < 8; j++) y[j] = f32x4{0.f, 0.f, 0.f, 0.f};
    {
        const int be = 15 - b16;
        unsigned fa = pair + (be & 3) * L_RSTR + 2 * (8 * q + (be & 12)) - 256 * mh + 64, fb = fa + 8;
        const unsigned ga = pair + L_U + 32 * b16 + 16 * q - 256 * mh + 288;
        asm volatile("" : "+v"(fb));
        v4u F[2][8], G[2][8];
#define CLOAD(cc, k) do { v2u lo_ = lds64(fa + 3968 - 64 * (cc) - 512 * (k)), hi_ = lds64(fb + 3968 - 64 * (cc) - 512 * (k)); \
                          F[(cc) & 1][k] = v4u{lo_.x, lo_.y, hi_.x, hi_.y}; G[(cc) & 1][k] = lds128(ga + 192 - 64 * (cc) + 512 * (k)); } while (0)
#pragma unroll
        for (int k = 0; k < 8; k++) CLOAD(0, k);
#pragma unroll
        for (int cc = 0; cc < 4; cc++) {
#pragma unroll
            for (int m = 0; m < 8; m++) {
                SB();
                if (cc < 3) CLOAD(cc + 1, m);
                SB();
#pragma unroll
                for (int k = 0; k < m; k++) if (k + m < 8) { y[k + m] = mfma16(F[cc & 1][k], G[cc & 1][m], y[k + m]); }
#pragma unroll
                for (int k = 0; k <= m; k++) if (k + m < 8) { y[k + m] = mfma16(F[cc & 1][m], G[cc & 1][k], y[k + m]); }
            }
        }
    }
    TS(8);
    if (mh) {
#pragma unroll
        for (int j = 0; j < 4; j++) { f32x4 t = y[j]; y[j] = y[4 + j]; y[4 + j] = t; asm volatile("" : "+v"(y[j]), "+v"(y[4 + j])); }
    }
    prog_raise(me, 4, lane);
    prog_wait1(other, 4);
#pragma unroll
    for (int j = 0; j < 4; j++) sts128(pair + mh * 4096 + j * 1024 + lane * 16, __builtin_bit_cast(v4u, y[4 + j]));
    prog_raise(me, 5, lane);
    TS(13);
    {
        const float ds = ch ? bfhi(dsk2) : bflo(dsk2);
        const unsigned stg = pair + 8192 + mh * 2048;
        v2u u[4];
#pragma unroll
        for (int j = 0; j < 4; j++) u[j] = lds64(pair + L_U + 512 + mh * 2048 + (256 * j + 16 * b16 + 4 * q) * 2);
#pragma unroll
        for (int j = 0; j < 4; j++) {
            y[j].x = __builtin_fmaf(bflo(u[j].x), ds, y[j].x); y[j].y = __builtin_fmaf(bfhi(u[j].x), ds, y[j].y);
            y[j].z = __builtin_fmaf(bflo(u[j].y), ds, y[j].z); y[j].w = __builtin_fmaf(bfhi(u[j].y), ds, y[j].w);
            asm volatile("" : "+v"(y[j]));
        }
        prog_wait1(other, 5);
        f32x4 t[4];
#pragma unroll
        for (int j = 0; j < 4; j++) t[j] = __builtin_bit_cast(f32x4, lds128(pair + (mh ^ 1) * 4096 + j * 1024 + lane * 16));
        SB();
#pragma unroll
        for (int j = 0; j < 4; j++) {
            t[j] += y[j];
            sts64(stg + (256 * j + 16 * b16 + 4 * q) * 2, v2u{min(pk(t[j].x, t[j].y), 0xfffffffeu), min(pk(t[j].z, t[j].w), 0xfffffffeu)});
        }
        prog_wait1(SY_PG + sw * 4, 11);
        st128(lds128(stg + lane * 16), rws, aout);
        st128(lds128(stg + 1024 + lane * 16), rws, aout + 1024);
    }
    TS(9);
}

__global__ __launch_bounds__(512, 2) void fused_kernel(Params P) {
    const int lane = threadIdx.x & 63, wv = rfl(threadIdx.x >> 6);
    const int xcd = blockIdx.x & 7, i = blockIdx.x >> 3;
    TSDECL;
    TS(0);
    if (wv == 5 && lane < 32) sts32(L_SYNC + lane * 4, 0u);
    asm volatile("s_waitcnt lgkmcnt(0)" : "+s"(P.x), "+s"(P.are), "+s"(P.aim), "+s"(P.bre), "+s"(P.bim), "+s"(P.cre), "+s"(P.cim), "+s"(P.logdt),
                 "+s"(P.dskip), "+s"(P.w1), "+s"(P.w2), "+s"(P.out), "+s"(P.ws)::"memory");
    if (i >= 28) {
        const rsrc_t rws = __builtin_amdgcn_make_buffer_rsrc((void*)P.ws, 0, WS_END, 0x00020000);
        const int sp = xcd * 4 + i - 28;
        ln_role(P, rws, sp, wv, lane TSPASS);
        TS(4);
        if (sp == 0 && wv == 0) {
            const u64 E = get_epoch();
            const char* fc = P.ws + WS_FH;
            poll2(fc + lane * 128, fc + (64 + lane) * 128, E);
            poll2(fc + (128 + lane) * 128, fc + (192 + (lane & 31)) * 128, E);
            poll1(P.ws + WS_FLN + (lane & 31) * 32, E);
            flag_set(P.ws + WS_EPOCH, E, lane);
            TS(5);
        }
    } else if (wv < 4) {
        gemm_role(P, xcd, i, wv, lane TSPASS);
    } else {
        ssm_role(P, xcd, i, wv - 4, lane TSPASS);
    }
    TSDUMP();
}

#if defined(__HIP_DEVICE_COMPILE__)
#pragma clang attribute pop
#endif

extern "C" void kernel_launch(void* const* d_in, const int* in_sizes, int n_in, void* d_out, int out_size, void* d_ws, size_t ws_size,
                              hipStream_t stream) {
    Params P;
    P.x = (const uint16_t*)d_in[0]; P.are = (const uint16_t*)d_in[1]; P.aim = (const uint16_t*)d_in[2];
    P.bre = (const uint16_t*)d_in[3]; P.bim = (const uint16_t*)d_in[4]; P.cre = (const uint16_t*)d_in[5]; P.cim = (const uint16_t*)d_in[6];
    P.logdt = (const uint16_t*)d_in[7]; P.dskip = (const uint16_t*)d_in[8]; P.w1 = (const uint16_t*)d_in[10]; P.w2 = (const uint16_t*)d_in[11];
    P.out = (uint16_t*)d_out; P.ws = (char*)d_ws + WS_BASE(ws_size);
    (void)hipFuncSetAttribute((const void*)fused_kernel, hipFuncAttributeMaxDynamicSharedMemorySize, L_TOTAL);
    fused_kernel<<<256, 512, L_TOTAL, stream>>>(P);
}
```
